```python
import torch

def _softmax(x, axis=-1):
    return torch.softmax(x, dim=axis)
def _gelu(x):
    return torch.nn.functional.gelu(x, approximate="tanh")
def _silu(x):
    return torch.nn.functional.silu(x)
def _sigmoid(x):
    return torch.sigmoid(x)
def _layernorm(x, axis=-1, eps=1e-5):
    m = x.mean(dim=axis, keepdim=True)
    v = x.var(dim=axis, keepdim=True, unbiased=False)
    return (x - m) / torch.sqrt(v + eps)
def _scatter_add(tgt, idx, src, axis=0):
    return tgt.clone().index_add_(axis, idx, src)
def _segment_sum(src, seg, n):
    out = torch.zeros((n,) + src.shape[1:], dtype=src.dtype)
    return out.index_add_(0, seg, src)
def _pad(x, pw):
    flat = tuple(v for pair in reversed(pw) for v in pair)
    return torch.nn.functional.pad(x, flat)

kernel_name = 'sequential_preLN__compressed_kv_cla__dense_activation_silu__decode'

def setup_inputs():

    _w = (lambda a: (lambda t: t.to(torch.bfloat16) if t.is_floating_point() else t)(torch.as_tensor(a)))
    def _A_setup():
        B, H, Sq, Sk, D, M = 4, 32, 1, 4096, 128, 640
        Dc = 4 * D
        return {
            "x": _w((lambda *s: torch.randn(*s, dtype=torch.bfloat16) * 0.05)(B, Sq, M)),
            "c_kv": _w((lambda *s: torch.randn(*s, dtype=torch.bfloat16) * 0.8)(B, Sk, Dc)),
            "w_q": _w((lambda *s: torch.randn(*s, dtype=torch.bfloat16) * 3.2)(M, H, D) * (M ** -0.5)),
            "w_uk": _w((lambda *s: torch.randn(*s, dtype=torch.bfloat16) * 3.2)(Dc, H, D) * (Dc ** -0.5)),
            "w_uv": _w((lambda *s: torch.randn(*s, dtype=torch.bfloat16) * 0.8)(Dc, H, D) * (Dc ** -0.5)),
            "w_o": _w((lambda *s: torch.randn(*s, dtype=torch.bfloat16) * 0.8)(H, D, M) * ((H * D) ** -0.5)),
        }
    _A_in = _A_setup()

    def _B_setup():
        B, S, D, F = 4, 1, 640, 1728
        return {
            "x": _w((lambda *s: torch.randn(*s, dtype=torch.bfloat16) * 0.05)(B, S, D)),
            "w1": _w((lambda *s: torch.randn(*s, dtype=torch.bfloat16) * 3.2)(D, F) * (D ** -0.5)),
            "w2": _w((lambda *s: torch.randn(*s, dtype=torch.bfloat16) * 0.8)(F, D) * (F ** -0.5)),
        }
    _B_in = _B_setup()

    return {
        "A_x": _A_in["x"],
        "A_c_kv": _A_in["c_kv"],
        "A_w_q": _A_in["w_q"],
        "A_w_uk": _A_in["w_uk"],
        "A_w_uv": _A_in["w_uv"],
        "A_w_o": _A_in["w_o"],
        "B_x": _B_in["x"],
        "B_w1": _B_in["w1"],
        "B_w2": _B_in["w2"],
    }

def reference(A_x, A_c_kv, A_w_q, A_w_uk, A_w_uv, A_w_o, B_x, B_w1, B_w2):
    def _A_ref(x, c_kv, w_q, w_uk, w_uv, w_o):
        B, Sq, M = x.shape[0], x.shape[1], x.shape[2]
        Sk = c_kv.shape[1]
        H, D = w_q.shape[1], w_q.shape[2]
        q = torch.einsum("bqm,mhd->bhqd", x, w_q)
        k = torch.einsum("bkc,chd->bhkd", c_kv, w_uk)
        v = torch.einsum("bkc,chd->bhkd", c_kv, w_uv)
        scores = torch.einsum("bhqd,bhkd->bhqk", q, k) * (D ** -0.5)
        off = Sk - Sq
        qi = torch.arange(Sq)[:, None]
        kj = torch.arange(Sk)[None, :]
        cmask = kj <= (qi + off)
        scores = torch.where(cmask[None, None], scores, -1e9)
        p = _softmax(scores, axis=-1)
        ctx = torch.einsum("bhqk,bhkd->bhqd", p, v)
        return torch.einsum("bhqd,hdm->bqm", ctx, w_o)

    def _B_ref(x, w1, w2):
        h = torch.einsum("bsd,df->bsf", x, w1)
        h = _silu(h)
        out = torch.einsum("bsf,fd->bsd", h, w2)
        return out

    x = A_x
    h = x + _A_ref(_layernorm(x, axis=-1), A_c_kv, A_w_q, A_w_uk, A_w_uv, A_w_o)
    h2 = h + _B_ref(_layernorm(h, axis=-1), B_w1, B_w2)
    return h2
```

```cpp
#include <hip/hip_runtime.h>
#include <hip/hip_bf16.h>
#include <stdint.h>

typedef unsigned short u16;
__device__ __forceinline__ float bf2f(u16 v){ return __uint_as_float(((unsigned)v)<<16); }
__device__ __forceinline__ u16 f2bf(float f){ unsigned u=__float_as_uint(f); u += 0x7FFFu + ((u>>16)&1u); return (u16)(u>>16); }

constexpr int B=4,H=32,SK=4096,D=128,M=640,DC=512,F=1728;

__device__ float block_sum(float v, float* sm){
  for(int o=32;o>0;o>>=1) v+=__shfl_xor(v,o);
  int w=threadIdx.x>>6; __syncthreads();
  if((threadIdx.x&63)==0) sm[w]=v; __syncthreads();
  float s=0; for(int i=0;i<(int)(blockDim.x>>6);i++) s+=sm[i]; return s;
}
__global__ void k_q(const u16* x,const u16* wq,float* q){
  __shared__ float xn[B][M]; __shared__ float sm[8]; __shared__ float red[4][B][64];
  for(int b=0;b<B;b++){
    float s=0; for(int i=threadIdx.x;i<M;i+=256) s+=bf2f(x[b*M+i]);
    float mean=block_sum(s,sm)/M; float v=0;
    for(int i=threadIdx.x;i<M;i+=256){ float d=bf2f(x[b*M+i])-mean; v+=d*d; }
    float var=block_sum(v,sm)/M; float r=rsqrtf(var+1e-5f);
    for(int i=threadIdx.x;i<M;i+=256) xn[b][i]=(bf2f(x[b*M+i])-mean)*r;
  }
  __syncthreads();
  int c=blockIdx.x*64+(threadIdx.x&63), g=threadIdx.x>>6;
  float a[B]={0,0,0,0};
  for(int k=g;k<M;k+=4){ float w=bf2f(wq[(size_t)k*4096+c]); for(int b=0;b<B;b++) a[b]+=xn[b][k]*w; }
  for(int b=0;b<B;b++) red[g][b][threadIdx.x&63]=a[b];
  __syncthreads();
  if(g==0) for(int b=0;b<B;b++){ int l=threadIdx.x; q[b*4096+c]=red[0][b][l]+red[1][b][l]+red[2][b][l]+red[3][b][l]; }
}
__global__ void k_qabs(const float* q,const u16* wuk,float* qabs){
  int h=blockIdx.x; int w=threadIdx.x>>6,l=threadIdx.x&63;
  for(int cc=w;cc<64;cc+=4){ int c=blockIdx.y*64+cc;
    float w0=bf2f(wuk[((size_t)c*H+h)*D+2*l]), w1=bf2f(wuk[((size_t)c*H+h)*D+2*l+1]);
    for(int b=0;b<B;b++){ float s=q[b*4096+h*D+2*l]*w0+q[b*4096+h*D+2*l+1]*w1;
      for(int o=32;o>0;o>>=1) s+=__shfl_xor(s,o);
      if(l==0) qabs[(b*H+h)*DC+c]=s*0.08838834764831845f; }
  }
}
__global__ void k_attn(const float* qabs,const u16* ckv,float* po,float* pm,float* pl){
  __shared__ float qa[H][DC]; __shared__ float sc[H][64];
  int b=blockIdx.x,s=blockIdx.y;
  for(int i=threadIdx.x;i<H*DC;i+=256) qa[i/DC][i%DC]=qabs[b*H*DC+i];
  __syncthreads();
  int w=threadIdx.x>>6,l=threadIdx.x&63;
  for(int kk=w;kk<64;kk+=4){ const u16* row=ckv+((size_t)b*SK+s*64+kk)*DC; float cv[8];
    for(int j=0;j<8;j++) cv[j]=bf2f(row[l*8+j]);
    for(int h=0;h<H;h++){ float a=0; for(int j=0;j<8;j++) a+=cv[j]*qa[h][l*8+j];
      for(int o=32;o>0;o>>=1) a+=__shfl_xor(a,o); if(l==0) sc[h][kk]=a; } }
  __syncthreads();
  for(int h=w;h<H;h+=4){ float v=sc[h][l]; float m=v; for(int o=32;o>0;o>>=1) m=fmaxf(m,__shfl_xor(m,o));
    float e=__expf(v-m); float t=e; for(int o=32;o>0;o>>=1) t+=__shfl_xor(t,o); sc[h][l]=e;
    if(l==0){ pm[(b*64+s)*H+h]=m; pl[(b*64+s)*H+h]=t; } }
  __syncthreads();
  for(int c=threadIdx.x;c<DC;c+=256){ float a[H]; for(int h=0;h<H;h++) a[h]=0;
    for(int kk=0;kk<64;kk++){ float cv=bf2f(ckv[((size_t)b*SK+s*64+kk)*DC+c]); for(int h=0;h<H;h++) a[h]+=sc[h][kk]*cv; }
    for(int h=0;h<H;h++) po[((size_t)(b*64+s)*H+h)*DC+c]=a[h]; }
}
__global__ void k_ctx(const float* po,const float* pm,const float* pl,const u16* wuv,float* ctx){
  __shared__ float o[DC]; __shared__ float red[4][D];
  int h=blockIdx.x,b=blockIdx.y;
  float m=-1e30f; for(int s=0;s<64;s++) m=fmaxf(m,pm[(b*64+s)*H+h]);
  float L=0; for(int s=0;s<64;s++) L+=pl[(b*64+s)*H+h]*__expf(pm[(b*64+s)*H+h]-m);
  int c=threadIdx.x; float a=0;
  for(int s=0;s<64;s++) a+=po[((size_t)(b*64+s)*H+h)*DC+c]*__expf(pm[(b*64+s)*H+h]-m);
  o[c]=a/L; __syncthreads();
  int d=threadIdx.x&127,g=threadIdx.x>>7; float t=0;
  for(int k=g;k<DC;k+=4) t+=o[k]*bf2f(wuv[((size_t)k*H+h)*D+d]);
  red[g][d]=t; __syncthreads();
  if(g==0) ctx[b*4096+h*D+d]=red[0][d]+red[1][d]+red[2][d]+red[3][d];
}
__global__ void k_wo(const float* ctx,const u16* wo,float* hp){
  __shared__ float red[4][B][64];
  int c=blockIdx.x*64+(threadIdx.x&63), g=threadIdx.x>>6; int k0=blockIdx.y*256;
  float a[B]={0,0,0,0};
  for(int k=k0+g;k<k0+256;k+=4){ float w=bf2f(wo[(size_t)k*M+c]); for(int b=0;b<B;b++) a[b]+=ctx[b*4096+k]*w; }
  for(int b=0;b<B;b++) red[g][b][threadIdx.x&63]=a[b];
  __syncthreads();
  if(g==0) for(int b=0;b<B;b++){ int l=threadIdx.x; hp[(blockIdx.y*B+b)*M+c]=red[0][b][l]+red[1][b][l]+red[2][b][l]+red[3][b][l]; }
}
__global__ void k_ffn1(const u16* x,const float* hp,const u16* w1,float* hout,float* h1){
  __shared__ float xn[B][M]; __shared__ float sm[8]; __shared__ float red[4][B][64];
  for(int b=0;b<B;b++){
    float s=0; for(int i=threadIdx.x;i<M;i+=256){ float v=bf2f(x[b*M+i]); for(int p=0;p<16;p++) v+=hp[(p*B+b)*M+i]; xn[b][i]=v; s+=v; if(blockIdx.x==0) hout[b*M+i]=v; }
    float mean=block_sum(s,sm)/M; float v=0;
    for(int i=threadIdx.x;i<M;i+=256){ float d=xn[b][i]-mean; v+=d*d; }
    float var=block_sum(v,sm)/M; float r=rsqrtf(var+1e-5f);
    for(int i=threadIdx.x;i<M;i+=256) xn[b][i]=(xn[b][i]-mean)*r;
  }
  __syncthreads();
  int c=blockIdx.x*64+(threadIdx.x&63), g=threadIdx.x>>6;
  float a[B]={0,0,0,0};
  for(int k=g;k<M;k+=4){ float w=bf2f(w1[(size_t)k*F+c]); for(int b=0;b<B;b++) a[b]+=xn[b][k]*w; }
  for(int b=0;b<B;b++) red[g][b][threadIdx.x&63]=a[b];
  __syncthreads();
  if(g==0) for(int b=0;b<B;b++){ int l=threadIdx.x; float t=red[0][b][l]+red[1][b][l]+red[2][b][l]+red[3][b][l]; h1[b*F+c]=t/(1.f+__expf(-t)); }
}
__global__ void k_ffn2(const float* hin,const float* h1,const u16* w2,u16* out){
  __shared__ float red[16][B][64];
  int c=blockIdx.x*64+(threadIdx.x&63), g=threadIdx.x>>6;
  float a[B]={0,0,0,0};
  for(int k=g;k<F;k+=16){ float w=bf2f(w2[(size_t)k*M+c]); for(int b=0;b<B;b++) a[b]+=h1[b*F+k]*w; }
  for(int b=0;b<B;b++) red[g][b][threadIdx.x&63]=a[b];
  __syncthreads();
  if(g==0) for(int b=0;b<B;b++){ int l=threadIdx.x; float t=hin[b*M+c]; for(int p=0;p<16;p++) t+=red[p][b][l]; out[b*M+c]=f2bf(t); }
}

extern "C" void kernel_launch(void* const* d_in, const int* in_sizes, int n_in, void* d_out, int out_size, void* d_ws, size_t ws_size, hipStream_t stream) {
  const u16* x=(const u16*)d_in[0]; const u16* ckv=(const u16*)d_in[1]; const u16* wq=(const u16*)d_in[2];
  const u16* wuk=(const u16*)d_in[3]; const u16* wuv=(const u16*)d_in[4]; const u16* wo=(const u16*)d_in[5];
  const u16* w1=(const u16*)d_in[7]; const u16* w2=(const u16*)d_in[8];
  float* ws=(float*)d_ws;
  float* q=ws; float* qabs=q+B*4096; float* pm=qabs+B*H*DC; float* pl=pm+B*64*H; float* ctx=pl+B*64*H;
  float* hp=ctx+B*4096; float* hout=hp+16*B*M; float* h1=hout+B*M; float* po=h1+B*F+1024;
  k_q<<<64,256,0,stream>>>(x,wq,q);
  k_qabs<<<dim3(H,8),256,0,stream>>>(q,wuk,qabs);
  k_attn<<<dim3(B,64),256,0,stream>>>(qabs,ckv,po,pm,pl);
  k_ctx<<<dim3(H,B),512,0,stream>>>(po,pm,pl,wuv,ctx);
  k_wo<<<dim3(10,16),256,0,stream>>>(ctx,wo,hp);
  k_ffn1<<<27,256,0,stream>>>(x,hp,w1,hout,h1);
  k_ffn2<<<10,1024,0,stream>>>(hout,h1,w2,(u16*)d_out);
}
```

```cpp
#define USE_NT 0
#define KMUL 0
#include <hip/hip_runtime.h>
#include <hip/hip_bf16.h>
#include <stdint.h>

typedef unsigned short u16;
__device__ __forceinline__ float bf2f(u16 v){ return __uint_as_float(((unsigned)v)<<16); }
__device__ __forceinline__ u16 f2bf(float f){ unsigned u=__float_as_uint(f); u += 0x7FFFu + ((u>>16)&1u); return (u16)(u>>16); }

constexpr int B=4,H=32,SK=4096,D=128,M=640,DC=512,F=1728;

__device__ float block_sum(float v, float* sm){
  for(int o=32;o>0;o>>=1) v+=__shfl_xor(v,o);
  int w=threadIdx.x>>6; __syncthreads();
  if((threadIdx.x&63)==0) sm[w]=v; __syncthreads();
  float s=0; for(int i=0;i<(int)(blockDim.x>>6);i++) s+=sm[i]; return s;
}
__global__ void k_q(const u16* x,const u16* wq,float* q){
  __shared__ float xn[B][M]; __shared__ float sm[8]; __shared__ float red[4][B][64];
  for(int b=0;b<B;b++){
    float s=0; for(int i=threadIdx.x;i<M;i+=256) s+=bf2f(x[b*M+i]);
    float mean=block_sum(s,sm)/M; float v=0;
    for(int i=threadIdx.x;i<M;i+=256){ float d=bf2f(x[b*M+i])-mean; v+=d*d; }
    float var=block_sum(v,sm)/M; float r=rsqrtf(var+1e-5f);
    for(int i=threadIdx.x;i<M;i+=256) xn[b][i]=(bf2f(x[b*M+i])-mean)*r;
  }
  __syncthreads();
  int c=blockIdx.x*64+(threadIdx.x&63), g=threadIdx.x>>6;
  float a[B]={0,0,0,0};
  for(int k=g;k<M;k+=4){ float w=bf2f(wq[(size_t)k*4096+c]); for(int b=0;b<B;b++) a[b]+=xn[b][k]*w; }
  for(int b=0;b<B;b++) red[g][b][threadIdx.x&63]=a[b];
  __syncthreads();
  if(g==0) for(int b=0;b<B;b++){ int l=threadIdx.x; q[b*4096+c]=red[0][b][l]+red[1][b][l]+red[2][b][l]+red[3][b][l]; }
}
__global__ void k_qabs(const float* q,const u16* wuk,float* qabs){
  int h=blockIdx.x; int w=threadIdx.x>>6,l=threadIdx.x&63;
  for(int cc=w;cc<64;cc+=4){ int c=blockIdx.y*64+cc;
    float w0=bf2f(wuk[((size_t)c*H+h)*D+2*l]), w1=bf2f(wuk[((size_t)c*H+h)*D+2*l+1]);
    for(int b=0;b<B;b++){ float s=q[b*4096+h*D+2*l]*w0+q[b*4096+h*D+2*l+1]*w1;
      for(int o=32;o>0;o>>=1) s+=__shfl_xor(s,o);
      if(l==0) qabs[(b*H+h)*DC+c]=s*0.08838834764831845f; }
  }
}
__global__ void k_attn(const float* qabs,const u16* ckv,float* po,float* pm,float* pl){
  __shared__ float qa[H][DC]; __shared__ float sc[H][64];
  int b=blockIdx.x,s=blockIdx.y;
  for(int i=threadIdx.x;i<H*DC;i+=256) qa[i/DC][i%DC]=qabs[b*H*DC+i];
  __syncthreads();
  int w=threadIdx.x>>6,l=threadIdx.x&63;
  for(int kk=w;kk<64;kk+=4){ const u16* row=ckv+((size_t)b*SK+s*64+kk)*DC; float cv[8];
    for(int j=0;j<8;j++) cv[j]=bf2f(row[l*8+j]);
    for(int h=0;h<H;h++){ float a=0; for(int j=0;j<8;j++) a+=cv[j]*qa[h][l*8+j];
      for(int o=32;o>0;o>>=1) a+=__shfl_xor(a,o); if(l==0) sc[h][kk]=a; } }
  __syncthreads();
  for(int h=w;h<H;h+=4){ float v=sc[h][l]; float m=v; for(int o=32;o>0;o>>=1) m=fmaxf(m,__shfl_xor(m,o));
    float e=__expf(v-m); float t=e; for(int o=32;o>0;o>>=1) t+=__shfl_xor(t,o); sc[h][l]=e;
    if(l==0){ pm[(b*64+s)*H+h]=m; pl[(b*64+s)*H+h]=t; } }
  __syncthreads();
  for(int c=threadIdx.x;c<DC;c+=256){ float a[H]; for(int h=0;h<H;h++) a[h]=0;
    for(int kk=0;kk<64;kk++){ float cv=bf2f(ckv[((size_t)b*SK+s*64+kk)*DC+c]); for(int h=0;h<H;h++) a[h]+=sc[h][kk]*cv; }
    for(int h=0;h<H;h++) po[((size_t)(b*64+s)*H+h)*DC+c]=a[h]; }
}
__global__ void k_ctx(const float* po,const float* pm,const float* pl,const u16* wuv,float* ctx){
  __shared__ float o[DC]; __shared__ float red[4][D];
  int h=blockIdx.x,b=blockIdx.y;
  float m=-1e30f; for(int s=0;s<64;s++) m=fmaxf(m,pm[(b*64+s)*H+h]);
  float L=0; for(int s=0;s<64;s++) L+=pl[(b*64+s)*H+h]*__expf(pm[(b*64+s)*H+h]-m);
  int c=threadIdx.x; float a=0;
  for(int s=0;s<64;s++) a+=po[((size_t)(b*64+s)*H+h)*DC+c]*__expf(pm[(b*64+s)*H+h]-m);
  o[c]=a/L; __syncthreads();
  int d=threadIdx.x&127,g=threadIdx.x>>7; float t=0;
  for(int k=g;k<DC;k+=4) t+=o[k]*bf2f(wuv[((size_t)k*H+h)*D+d]);
  red[g][d]=t; __syncthreads();
  if(g==0) ctx[b*4096+h*D+d]=red[0][d]+red[1][d]+red[2][d]+red[3][d];
}
__global__ void k_wo(const float* ctx,const u16* wo,float* hp){
  __shared__ float red[4][B][64];
  int c=blockIdx.x*64+(threadIdx.x&63), g=threadIdx.x>>6; int k0=blockIdx.y*256;
  float a[B]={0,0,0,0};
  for(int k=k0+g;k<k0+256;k+=4){ float w=bf2f(wo[(size_t)k*M+c]); for(int b=0;b<B;b++) a[b]+=ctx[b*4096+k]*w; }
  for(int b=0;b<B;b++) red[g][b][threadIdx.x&63]=a[b];
  __syncthreads();
  if(g==0) for(int b=0;b<B;b++){ int l=threadIdx.x; hp[(blockIdx.y*B+b)*M+c]=red[0][b][l]+red[1][b][l]+red[2][b][l]+red[3][b][l]; }
}
__global__ void k_ffn1(const u16* x,const float* hp,const u16* w1,float* hout,float* h1){
  __shared__ float xn[B][M]; __shared__ float sm[8]; __shared__ float red[4][B][64];
  for(int b=0;b<B;b++){
    float s=0; for(int i=threadIdx.x;i<M;i+=256){ float v=bf2f(x[b*M+i]); for(int p=0;p<16;p++) v+=hp[(p*B+b)*M+i]; xn[b][i]=v; s+=v; if(blockIdx.x==0) hout[b*M+i]=v; }
    float mean=block_sum(s,sm)/M; float v=0;
    for(int i=threadIdx.x;i<M;i+=256){ float d=xn[b][i]-mean; v+=d*d; }
    float var=block_sum(v,sm)/M; float r=rsqrtf(var+1e-5f);
    for(int i=threadIdx.x;i<M;i+=256) xn[b][i]=(xn[b][i]-mean)*r;
  }
  __syncthreads();
  int c=blockIdx.x*64+(threadIdx.x&63), g=threadIdx.x>>6;
  float a[B]={0,0,0,0};
  for(int k=g;k<M;k+=4){ float w=bf2f(w1[(size_t)k*F+c]); for(int b=0;b<B;b++) a[b]+=xn[b][k]*w; }
  for(int b=0;b<B;b++) red[g][b][threadIdx.x&63]=a[b];
  __syncthreads();
  if(g==0) for(int b=0;b<B;b++){ int l=threadIdx.x; float t=red[0][b][l]+red[1][b][l]+red[2][b][l]+red[3][b][l]; h1[b*F+c]=t/(1.f+__expf(-t)); }
}
__global__ void k_ffn2(const float* hin,const float* h1,const u16* w2,u16* out){
  __shared__ float red[16][B][64];
  int c=blockIdx.x*64+(threadIdx.x&63), g=threadIdx.x>>6;
  float a[B]={0,0,0,0};
  for(int k=g;k<F;k+=16){ float w=bf2f(w2[(size_t)k*M+c]); for(int b=0;b<B;b++) a[b]+=h1[b*F+k]*w; }
  for(int b=0;b<B;b++) red[g][b][threadIdx.x&63]=a[b];
  __syncthreads();
  if(g==0) for(int b=0;b<B;b++){ int l=threadIdx.x; float t=hin[b*M+c]; for(int p=0;p<16;p++) t+=red[p][b][l]; out[b*M+c]=f2bf(t); }
}

typedef unsigned v4u __attribute__((ext_vector_type(4)));
#define LD(p) (*(p))
__global__ void __launch_bounds__(512) k_stream(const v4u* __restrict__ a0,const v4u* __restrict__ a1,const v4u* __restrict__ a2,const v4u* __restrict__ a3,const v4u* __restrict__ a4,const v4u* __restrict__ a5,const v4u* __restrict__ a6, unsigned* ws){
  unsigned long long t0=__builtin_amdgcn_s_memrealtime();
  const unsigned n2=640*4096/8, n6=640*1728/8;
  unsigned tid=blockIdx.x*512+threadIdx.x; const unsigned NT=256*512;
  v4u acc={0,0,0,0};
  v4u r[24];
  int c=0;
#pragma unroll
  for(int i=0;i<8;i++) r[c++]=LD(a0+tid+i*NT);
#pragma unroll
  for(int i=0;i<2;i++) r[c++]=LD(a2+tid+i*NT);
#pragma unroll
  for(int i=0;i<2;i++) r[c++]=LD(a3+tid+i*NT);
#pragma unroll
  for(int i=0;i<3;i++){ unsigned j=tid+i*NT; j = j<n2? j: tid; r[c++]= LD(a1+j); }
#pragma unroll
  for(int i=0;i<3;i++){ unsigned j=tid+i*NT; j = j<n2? j: tid; r[c++]= LD(a4+j); }
#pragma unroll
  for(int i=0;i<2;i++){ unsigned j=tid+i*NT; j = j<n6? j: tid; r[c++]= LD(a5+j); }
#pragma unroll
  for(int i=0;i<2;i++){ unsigned j=tid+i*NT; j = j<n6? j: tid; r[c++]= LD(a6+j); }
  __builtin_amdgcn_sched_barrier(0);
#pragma unroll
  for(int i=0;i<22;i++) acc^=r[i];
  unsigned s=acc.x^acc.y^acc.z^acc.w;
  if(s==0x12345678u) ws[tid]=s;
  asm volatile("s_waitcnt vmcnt(0)":::"memory");
  unsigned long long t1=__builtin_amdgcn_s_memrealtime();
  unsigned long long tend=t1+(t1-t0)*KMUL;
  while(__builtin_amdgcn_s_memrealtime()<tend) __builtin_amdgcn_s_sleep(8);
}

extern "C" void kernel_launch(void* const* d_in, const int* in_sizes, int n_in, void* d_out, int out_size, void* d_ws, size_t ws_size, hipStream_t stream) {
  const u16* x=(const u16*)d_in[0]; const u16* ckv=(const u16*)d_in[1]; const u16* wq=(const u16*)d_in[2];
  const u16* wuk=(const u16*)d_in[3]; const u16* wuv=(const u16*)d_in[4]; const u16* wo=(const u16*)d_in[5];
  const u16* w1=(const u16*)d_in[7]; const u16* w2=(const u16*)d_in[8];
  float* ws=(float*)d_ws;
  float* q=ws; float* qabs=q+B*4096; float* pm=qabs+B*H*DC; float* pl=pm+B*64*H; float* ctx=pl+B*64*H;
  float* hp=ctx+B*4096; float* hout=hp+16*B*M; float* h1=hout+B*M; float* po=h1+B*F+1024;
  k_stream<<<256,512,0,stream>>>((const v4u*)d_in[1],(const v4u*)d_in[2],(const v4u*)d_in[3],(const v4u*)d_in[4],(const v4u*)d_in[5],(const v4u*)d_in[7],(const v4u*)d_in[8],(unsigned*)d_ws+(1<<24));
  k_q<<<64,256,0,stream>>>(x,wq,q);
  k_qabs<<<dim3(H,8),256,0,stream>>>(q,wuk,qabs);
  k_attn<<<dim3(B,64),256,0,stream>>>(qabs,ckv,po,pm,pl);
  k_ctx<<<dim3(H,B),512,0,stream>>>(po,pm,pl,wuv,ctx);
  k_wo<<<dim3(10,16),256,0,stream>>>(ctx,wo,hp);
  k_ffn1<<<27,256,0,stream>>>(x,hp,w1,hout,h1);
  k_ffn2<<<10,1024,0,stream>>>(hout,h1,w2,(u16*)d_out);
}
```

```cpp
#define MODE 3
#define KMUL 0
#include <hip/hip_runtime.h>
#include <hip/hip_bf16.h>
#include <stdint.h>

typedef unsigned short u16;
__device__ __forceinline__ float bf2f(u16 v){ return __uint_as_float(((unsigned)v)<<16); }
__device__ __forceinline__ u16 f2bf(float f){ unsigned u=__float_as_uint(f); u += 0x7FFFu + ((u>>16)&1u); return (u16)(u>>16); }

constexpr int B=4,H=32,SK=4096,D=128,M=640,DC=512,F=1728;

__device__ float block_sum(float v, float* sm){
  for(int o=32;o>0;o>>=1) v+=__shfl_xor(v,o);
  int w=threadIdx.x>>6; __syncthreads();
  if((threadIdx.x&63)==0) sm[w]=v; __syncthreads();
  float s=0; for(int i=0;i<(int)(blockDim.x>>6);i++) s+=sm[i]; return s;
}
__global__ void k_q(const u16* x,const u16* wq,float* q){
  __shared__ float xn[B][M]; __shared__ float sm[8]; __shared__ float red[4][B][64];
  for(int b=0;b<B;b++){
    float s=0; for(int i=threadIdx.x;i<M;i+=256) s+=bf2f(x[b*M+i]);
    float mean=block_sum(s,sm)/M; float v=0;
    for(int i=threadIdx.x;i<M;i+=256){ float d=bf2f(x[b*M+i])-mean; v+=d*d; }
    float var=block_sum(v,sm)/M; float r=rsqrtf(var+1e-5f);
    for(int i=threadIdx.x;i<M;i+=256) xn[b][i]=(bf2f(x[b*M+i])-mean)*r;
  }
  __syncthreads();
  int c=blockIdx.x*64+(threadIdx.x&63), g=threadIdx.x>>6;
  float a[B]={0,0,0,0};
  for(int k=g;k<M;k+=4){ float w=bf2f(wq[(size_t)k*4096+c]); for(int b=0;b<B;b++) a[b]+=xn[b][k]*w; }
  for(int b=0;b<B;b++) red[g][b][threadIdx.x&63]=a[b];
  __syncthreads();
  if(g==0) for(int b=0;b<B;b++){ int l=threadIdx.x; q[b*4096+c]=red[0][b][l]+red[1][b][l]+red[2][b][l]+red[3][b][l]; }
}
__global__ void k_qabs(const float* q,const u16* wuk,float* qabs){
  int h=blockIdx.x; int w=threadIdx.x>>6,l=threadIdx.x&63;
  for(int cc=w;cc<64;cc+=4){ int c=blockIdx.y*64+cc;
    float w0=bf2f(wuk[((size_t)c*H+h)*D+2*l]), w1=bf2f(wuk[((size_t)c*H+h)*D+2*l+1]);
    for(int b=0;b<B;b++){ float s=q[b*4096+h*D+2*l]*w0+q[b*4096+h*D+2*l+1]*w1;
      for(int o=32;o>0;o>>=1) s+=__shfl_xor(s,o);
      if(l==0) qabs[(b*H+h)*DC+c]=s*0.08838834764831845f; }
  }
}
__global__ void k_attn(const float* qabs,const u16* ckv,float* po,float* pm,float* pl){
  __shared__ float qa[H][DC]; __shared__ float sc[H][64];
  int b=blockIdx.x,s=blockIdx.y;
  for(int i=threadIdx.x;i<H*DC;i+=256) qa[i/DC][i%DC]=qabs[b*H*DC+i];
  __syncthreads();
  int w=threadIdx.x>>6,l=threadIdx.x&63;
  for(int kk=w;kk<64;kk+=4){ const u16* row=ckv+((size_t)b*SK+s*64+kk)*DC; float cv[8];
    for(int j=0;j<8;j++) cv[j]=bf2f(row[l*8+j]);
    for(int h=0;h<H;h++){ float a=0; for(int j=0;j<8;j++) a+=cv[j]*qa[h][l*8+j];
      for(int o=32;o>0;o>>=1) a+=__shfl_xor(a,o); if(l==0) sc[h][kk]=a; } }
  __syncthreads();
  for(int h=w;h<H;h+=4){ float v=sc[h][l]; float m=v; for(int o=32;o>0;o>>=1) m=fmaxf(m,__shfl_xor(m,o));
    float e=__expf(v-m); float t=e; for(int o=32;o>0;o>>=1) t+=__shfl_xor(t,o); sc[h][l]=e;
    if(l==0){ pm[(b*64+s)*H+h]=m; pl[(b*64+s)*H+h]=t; } }
  __syncthreads();
  for(int c=threadIdx.x;c<DC;c+=256){ float a[H]; for(int h=0;h<H;h++) a[h]=0;
    for(int kk=0;kk<64;kk++){ float cv=bf2f(ckv[((size_t)b*SK+s*64+kk)*DC+c]); for(int h=0;h<H;h++) a[h]+=sc[h][kk]*cv; }
    for(int h=0;h<H;h++) po[((size_t)(b*64+s)*H+h)*DC+c]=a[h]; }
}
__global__ void k_ctx(const float* po,const float* pm,const float* pl,const u16* wuv,float* ctx){
  __shared__ float o[DC]; __shared__ float red[4][D];
  int h=blockIdx.x,b=blockIdx.y;
  float m=-1e30f; for(int s=0;s<64;s++) m=fmaxf(m,pm[(b*64+s)*H+h]);
  float L=0; for(int s=0;s<64;s++) L+=pl[(b*64+s)*H+h]*__expf(pm[(b*64+s)*H+h]-m);
  int c=threadIdx.x; float a=0;
  for(int s=0;s<64;s++) a+=po[((size_t)(b*64+s)*H+h)*DC+c]*__expf(pm[(b*64+s)*H+h]-m);
  o[c]=a/L; __syncthreads();
  int d=threadIdx.x&127,g=threadIdx.x>>7; float t=0;
  for(int k=g;k<DC;k+=4) t+=o[k]*bf2f(wuv[((size_t)k*H+h)*D+d]);
  red[g][d]=t; __syncthreads();
  if(g==0) ctx[b*4096+h*D+d]=red[0][d]+red[1][d]+red[2][d]+red[3][d];
}
__global__ void k_wo(const float* ctx,const u16* wo,float* hp){
  __shared__ float red[4][B][64];
  int c=blockIdx.x*64+(threadIdx.x&63), g=threadIdx.x>>6; int k0=blockIdx.y*256;
  float a[B]={0,0,0,0};
  for(int k=k0+g;k<k0+256;k+=4){ float w=bf2f(wo[(size_t)k*M+c]); for(int b=0;b<B;b++) a[b]+=ctx[b*4096+k]*w; }
  for(int b=0;b<B;b++) red[g][b][threadIdx.x&63]=a[b];
  __syncthreads();
  if(g==0) for(int b=0;b<B;b++){ int l=threadIdx.x; hp[(blockIdx.y*B+b)*M+c]=red[0][b][l]+red[1][b][l]+red[2][b][l]+red[3][b][l]; }
}
__global__ void k_ffn1(const u16* x,const float* hp,const u16* w1,float* hout,float* h1){
  __shared__ float xn[B][M]; __shared__ float sm[8]; __shared__ float red[4][B][64];
  for(int b=0;b<B;b++){
    float s=0; for(int i=threadIdx.x;i<M;i+=256){ float v=bf2f(x[b*M+i]); for(int p=0;p<16;p++) v+=hp[(p*B+b)*M+i]; xn[b][i]=v; s+=v; if(blockIdx.x==0) hout[b*M+i]=v; }
    float mean=block_sum(s,sm)/M; float v=0;
    for(int i=threadIdx.x;i<M;i+=256){ float d=xn[b][i]-mean; v+=d*d; }
    float var=block_sum(v,sm)/M; float r=rsqrtf(var+1e-5f);
    for(int i=threadIdx.x;i<M;i+=256) xn[b][i]=(xn[b][i]-mean)*r;
  }
  __syncthreads();
  int c=blockIdx.x*64+(threadIdx.x&63), g=threadIdx.x>>6;
  float a[B]={0,0,0,0};
  for(int k=g;k<M;k+=4){ float w=bf2f(w1[(size_t)k*F+c]); for(int b=0;b<B;b++) a[b]+=xn[b][k]*w; }
  for(int b=0;b<B;b++) red[g][b][threadIdx.x&63]=a[b];
  __syncthreads();
  if(g==0) for(int b=0;b<B;b++){ int l=threadIdx.x; float t=red[0][b][l]+red[1][b][l]+red[2][b][l]+red[3][b][l]; h1[b*F+c]=t/(1.f+__expf(-t)); }
}
__global__ void k_ffn2(const float* hin,const float* h1,const u16* w2,u16* out){
  __shared__ float red[16][B][64];
  int c=blockIdx.x*64+(threadIdx.x&63), g=threadIdx.x>>6;
  float a[B]={0,0,0,0};
  for(int k=g;k<F;k+=16){ float w=bf2f(w2[(size_t)k*M+c]); for(int b=0;b<B;b++) a[b]+=h1[b*F+k]*w; }
  for(int b=0;b<B;b++) red[g][b][threadIdx.x&63]=a[b];
  __syncthreads();
  if(g==0) for(int b=0;b<B;b++){ int l=threadIdx.x; float t=hin[b*M+c]; for(int p=0;p<16;p++) t+=red[p][b][l]; out[b*M+c]=f2bf(t); }
}

typedef unsigned long long u64;
__device__ __forceinline__ u64 rt(){ u64 t; asm volatile("s_memrealtime %0\n\ts_waitcnt lgkmcnt(0)":"=s"(t)::"memory"); return t; }
#define R8(x) x x x x x x x x
#define R64(x) R8(R8(x))
#define JMP1K "s_branch 256\n\t" R64("s_nop 0\n\ts_nop 0\n\ts_nop 0\n\ts_nop 0\n\t")
__global__ void __launch_bounds__(256) k_probe(const unsigned* __restrict__ a0, unsigned* ws){
  u64 t0=rt();
  u64 t1=rt();
  u64 tend=t1+(t1-t0)*KMUL;
  while(__builtin_amdgcn_s_memrealtime()<tend) __builtin_amdgcn_s_sleep(8);
}

extern "C" void kernel_launch(void* const* d_in, const int* in_sizes, int n_in, void* d_out, int out_size, void* d_ws, size_t ws_size, hipStream_t stream) {
  const u16* x=(const u16*)d_in[0]; const u16* ckv=(const u16*)d_in[1]; const u16* wq=(const u16*)d_in[2];
  const u16* wuk=(const u16*)d_in[3]; const u16* wuv=(const u16*)d_in[4]; const u16* wo=(const u16*)d_in[5];
  const u16* w1=(const u16*)d_in[7]; const u16* w2=(const u16*)d_in[8];
  float* ws=(float*)d_ws;
  float* q=ws; float* qabs=q+B*4096; float* pm=qabs+B*H*DC; float* pl=pm+B*64*H; float* ctx=pl+B*64*H;
  float* hp=ctx+B*4096; float* hout=hp+16*B*M; float* h1=hout+B*M; float* po=h1+B*F+1024;
  k_probe<<<256,256,0,stream>>>((const unsigned*)d_in[1],(unsigned*)d_ws+(1<<24));
  k_q<<<64,256,0,stream>>>(x,wq,q);
  k_qabs<<<dim3(H,8),256,0,stream>>>(q,wuk,qabs);
  k_attn<<<dim3(B,64),256,0,stream>>>(qabs,ckv,po,pm,pl);
  k_ctx<<<dim3(H,B),512,0,stream>>>(po,pm,pl,wuv,ctx);
  k_wo<<<dim3(10,16),256,0,stream>>>(ctx,wo,hp);
  k_ffn1<<<27,256,0,stream>>>(x,hp,w1,hout,h1);
  k_ffn2<<<10,1024,0,stream>>>(hout,h1,w2,(u16*)d_out);
}
```

```cpp
#include <hip/hip_runtime.h>
#include <stdint.h>

namespace {
typedef unsigned short u16;
typedef unsigned long long u64;
typedef unsigned v4u __attribute__((ext_vector_type(4)));
typedef unsigned v8u __attribute__((ext_vector_type(8)));
typedef unsigned v2u __attribute__((ext_vector_type(2)));
typedef float f32x2 __attribute__((ext_vector_type(2)));
typedef float f32x4 __attribute__((ext_vector_type(4)));
typedef __bf16 bf16x2 __attribute__((ext_vector_type(2)));
typedef __bf16 bf16x8 __attribute__((ext_vector_type(8)));
typedef short s16x4 __attribute__((ext_vector_type(4)));
#define LAS __attribute__((address_space(3)))
#define GAS __attribute__((address_space(1)))
typedef __amdgpu_buffer_rsrc_t rsrc_t;
#define DEVI __device__ __forceinline__

constexpr unsigned WS_EPOCH = 0;
constexpr unsigned WS_FLAGS = 1024;
constexpr unsigned WS_QG = 0x100000 + 0x20000;
constexpr unsigned WS_QABS = 0x20000;
constexpr unsigned WS_STATS = 0x170000;
constexpr unsigned WS_HP = 0x48000;
constexpr unsigned WS_CTX = 0x60000;
constexpr unsigned WS_FP = 0x80000;
constexpr unsigned WS_HINT = 0x190000;
constexpr unsigned WS_OP = 0x200000;
constexpr unsigned OP_STRIDE = 32768 + 128;
constexpr unsigned WS_BYTES = WS_OP + 128 * OP_STRIDE;
constexpr unsigned SENT = 0xFFFFFFFFu;
constexpr unsigned SPIN_CAP = 1u << 15;
constexpr int SC1 = 16, NT = 2;
constexpr unsigned HINT_B = 0x8000;
constexpr int LDS_BYTES = 160 * 1024;

DEVI rsrc_t mk_rsrc(const void* p, unsigned bytes) { return __builtin_amdgcn_make_buffer_rsrc((void*)p, (short)0, (int)bytes, 0x00020000); }
DEVI unsigned rfl(unsigned v) { return __builtin_amdgcn_readfirstlane(v); }
template <typename T> DEVI LAS T* lp(unsigned off) { return (LAS T*)(uintptr_t)off; }
DEVI void barrier() {
  asm volatile("s_waitcnt lgkmcnt(0)" ::: "memory");
  __builtin_amdgcn_s_barrier();
  asm volatile("" ::: "memory");
}
#define VMWAIT(n) asm volatile("s_waitcnt vmcnt(" #n ")" ::: "memory")

DEVI void dma(rsrc_t rs, unsigned voff, unsigned soff, unsigned lds) {
  asm volatile("s_mov_b32 m0, %0\n\ts_nop 0\n\tbuffer_load_dwordx4 %1, %2, %3 offen lds" ::"s"(lds), "v"(voff), "s"(rs), "s"(soff) : "memory");
}
DEVI void dma_sc1(rsrc_t rs, unsigned voff, unsigned soff, unsigned lds) {
  asm volatile("s_mov_b32 m0, %0\n\ts_nop 0\n\tbuffer_load_dwordx4 %1, %2, %3 offen sc1 lds" ::"s"(lds), "v"(voff), "s"(rs), "s"(soff) : "memory");
}
DEVI v2u ld8_sc1_asm(rsrc_t rs, unsigned voff, unsigned soff) {
  v2u r;
  asm volatile("buffer_load_dwordx2 %0, %1, %2, %3 offen sc1" : "=&v"(r) : "v"(voff), "s"(rs), "s"(soff) : "memory");
  return r;
}
DEVI unsigned ld4_asm(rsrc_t rs, unsigned voff, unsigned soff) {
  unsigned r;
  asm volatile("buffer_load_dword %0, %1, %2, %3 offen" : "=&v"(r) : "v"(voff), "s"(rs), "s"(soff) : "memory");
  return r;
}
DEVI v4u ldg16(rsrc_t rs, unsigned voff, unsigned soff) { return __builtin_amdgcn_raw_buffer_load_b128(rs, voff, soff, SC1); }
DEVI v2u ldg8(rsrc_t rs, unsigned voff, unsigned soff) { return __builtin_amdgcn_raw_buffer_load_b64(rs, voff, soff, SC1); }
DEVI void stg16(v4u v, rsrc_t rs, unsigned voff, unsigned soff) { __builtin_amdgcn_raw_buffer_store_b128(v, rs, voff, soff, SC1); }
DEVI void stg8(v2u v, rsrc_t rs, unsigned voff, unsigned soff) { __builtin_amdgcn_raw_buffer_store_b64(v, rs, voff, soff, SC1); }
DEVI void stg4(unsigned v, rsrc_t rs, unsigned voff, unsigned soff) { __builtin_amdgcn_raw_buffer_store_b32(v, rs, voff, soff, SC1); }

DEVI unsigned pk_bf16(float a, float b) {
  f32x2 f = {a, b};
  return __builtin_bit_cast(unsigned, __builtin_convertvector(f, bf16x2));
}
DEVI float bf_lo(unsigned u) { return __uint_as_float(u << 16); }
DEVI float bf_hi(unsigned u) { return __uint_as_float(u & 0xFFFF0000u); }
DEVI unsigned san(unsigned u) { return u < 0xFFFFFFFEu ? u : 0xFFFFFFFEu; }
DEVI unsigned umax4(v4u a) { return max(max(a.x, a.y), max(a.z, a.w)); }
DEVI f32x4 mfma16(v4u a, v4u b, f32x4 c) {
  return __builtin_amdgcn_mfma_f32_16x16x32_bf16(__builtin_bit_cast(bf16x8, a), __builtin_bit_cast(bf16x8, b), c, 0, 0, 0);
}
template <int CTRL> DEVI float dpp(float v) {
  return __int_as_float(__builtin_amdgcn_update_dpp(0, __float_as_int(v), CTRL, 0xF, 0xF, true));
}
template <int CTRL> DEVI unsigned dppu(unsigned v) { return (unsigned)__builtin_amdgcn_update_dpp(0, (int)v, CTRL, 0xF, 0xF, true); }
constexpr int QP_X1 = 0xB1, QP_X2 = 0x4E;
constexpr int ROR4 = 0x124, ROR8 = 0x128;
DEVI float quad_sum(float v) {
  v += dpp<QP_X1>(v);
  v += dpp<QP_X2>(v);
  return v;
}
DEVI float sum16(float v) {
  auto r = __builtin_amdgcn_permlane16_swap(__float_as_uint(v), __float_as_uint(v), false, false);
  return __uint_as_float(r[0]) + __uint_as_float(r[1]);
}
DEVI float sum32(float v) {
  auto r = __builtin_amdgcn_permlane32_swap(__float_as_uint(v), __float_as_uint(v), false, false);
  return __uint_as_float(r[0]) + __uint_as_float(r[1]);
}
DEVI v4u tr_frag(unsigned addr, unsigned step) {
  s16x4 a = __builtin_amdgcn_ds_read_tr16_b64_v4i16(lp<s16x4>(addr));
  s16x4 b = __builtin_amdgcn_ds_read_tr16_b64_v4i16(lp<s16x4>(addr + step));
  v2u x = __builtin_bit_cast(v2u, a), y = __builtin_bit_cast(v2u, b);
  return v4u{x.x, x.y, y.x, y.y};
}


DEVI float sum4(f32x4 v) {
  float a = v[0] + v[1], b = v[2] + v[3];
  asm("" : "+v"(a));
  return a + b;
}
#define PIN4(a) asm volatile("" : "+v"(a))
template <int N> DEVI void snooze() {
#pragma unroll
  for (int i = 0; i < N / 100; ++i) __builtin_amdgcn_s_sleep(100);
  if (N % 100) __builtin_amdgcn_s_sleep(N % 100);
}

constexpr int A_DELAY = 16, H1_DELAY = 4, H6_DELAY = 3, WO_DELAY = 6, F_DELAY = 50, PF_CKV = 4;
constexpr int T_Q = 450, T_L = 495, D_S = 150, D_4 = 100, D_5 = 100, D_F = 100, LAG_CAP = 2000;
DEVI unsigned swz(unsigned r) { return (((r ^ ((r >> 3) * 12)) << 1) | (r >> 3)) & 15; }
DEVI float max2(float a, float b) { return __builtin_amdgcn_fmed3f(a, b, __builtin_inff()); }
DEVI float max4(float a, float b, float c, float d) { return max2(max2(a, b), max2(c, d)); }
DEVI float fold16(float a, float b) {
  auto r = __builtin_amdgcn_permlane16_swap(__float_as_uint(a), __float_as_uint(b), false, false);
  return __uint_as_float(r[0]) + __uint_as_float(r[1]);
}
DEVI void l2_pull(rsrc_t rs, unsigned voff, unsigned soff, unsigned lds) {
  asm volatile("s_mov_b32 m0, %0\n\ts_nop 0\n\tbuffer_load_dword %1, %2, %3 offen lds" ::"s"(lds), "v"(voff), "s"(rs), "s"(soff) : "memory");
}
DEVI void raw_barrier() {
  __builtin_amdgcn_s_barrier();
  asm volatile("" ::: "memory");
}
DEVI void wait_hint(rsrc_t rws, unsigned voff, unsigned soff, unsigned tg, bool need = true) {
#pragma nounroll
  for (unsigned sp = 0; sp < SPIN_CAP; ++sp) {
    asm volatile("" ::: "memory");
    if (need) need = __builtin_amdgcn_raw_buffer_load_b32(rws, voff, soff, SC1) != tg;
    if (!__any(need)) break;
    __builtin_amdgcn_s_sleep(1);
  }
}
DEVI int wave_max(int v) {
  v = max(v, (int)dppu<QP_X1>(v));
  v = max(v, (int)dppu<QP_X2>(v));
  v = max(v, (int)dppu<ROR4>(v));
  v = max(v, (int)dppu<ROR8>(v));
  return max(max(__builtin_amdgcn_readlane(v, 0), __builtin_amdgcn_readlane(v, 16)),
             max(__builtin_amdgcn_readlane(v, 32), __builtin_amdgcn_readlane(v, 48)));
}
DEVI bool flags_ok(v4u a, v4u b, unsigned tlo, unsigned thi) { return ((a.x ^ tlo) | (b.x ^ tlo) | (a.y ^ thi) | (b.y ^ thi)) == 0; }
DEVI unsigned now32() { return (unsigned)__builtin_amdgcn_s_memrealtime(); }
DEVI void announce(rsrc_t rws, unsigned so, unsigned tg) { stg8(v2u{now32(), tg}, rws, WS_HINT, so); }
DEVI void paced_wait(rsrc_t rws, unsigned voff, unsigned tg, bool need, unsigned t_ref, unsigned delay, v2u hv = v2u{0, 0}) {
  int late = -0x40000000;
#pragma nounroll
  for (unsigned sp = 0; sp < SPIN_CAP; ++sp) {
    if (need && hv.y == tg) late = (int)(hv.x - t_ref), need = false;
    if (!__any(need)) break;
    asm volatile("" ::: "memory");
    if (sp) __builtin_amdgcn_s_sleep(2);
    if (need) hv = ldg8(rws, WS_HINT + voff, 0);
  }
  const int l = wave_max(late);
#pragma nounroll
  for (unsigned sp = 0; sp < SPIN_CAP && (int)(now32() - t_ref) < l + (int)delay; ++sp) __builtin_amdgcn_s_sleep(1);
}
#define GET_TAG(ev)                                               \
  {                                                               \
    const u64 tag = (((u64)rfl(ev.y) << 32) | rfl(ev.x)) + 1;     \
    tlo = (unsigned)tag, thi = (unsigned)(tag >> 32);             \
    tg = tlo & 0x7FFFFFFFu;                                       \
  }

__global__ void __launch_bounds__(512, 2)
fused_kernel(const u16* a_x, const u16* a_ckv, const u16* a_wq, const u16* a_wuk, const u16* a_wuv, const u16* a_wo, const u16* a_w1,
             const u16* a_w2, u16* a_out, char* a_ws) {
  v8u k0, k1;
  v4u k2;
  u64 t_s;
  asm volatile(
      ".Lfused_pf_begin:\n\ts_load_dwordx8 %1, %4, 0x0\n\ts_load_dwordx8 %2, %4, 0x20\n\ts_load_dwordx4 %3, %4, 0x40\n\ts_memrealtime %0"
      : "=&s"(t_s), "=&s"(k0), "=&s"(k1), "=&s"(k2)
      : "s"(__builtin_amdgcn_kernarg_segment_ptr()));
  if (blockIdx.x >= 128 && threadIdx.x < 32) {
    u64 pc;
    unsigned back, len;
    asm volatile("s_getpc_b64 %0\n.Lfused_pf_here:\n\ts_mov_b32 %1, .Lfused_pf_here-.Lfused_pf_begin\n\ts_mov_b32 %2, .Lfused_pf_end-.Lfused_pf_begin"
                 : "=s"(pc), "=s"(back), "=s"(len));
    asm volatile("s_mov_b32 m0, 0x27000\n\ts_nop 0\n\tbuffer_load_dword %0, %1, %2 offen lds" ::"v"(threadIdx.x * 1024),
                 "s"(mk_rsrc((const void*)(pc - back), len)), "s"(((blockIdx.x >> 3) & 15) * 64)
                 : "memory");
  }
  const unsigned lane = threadIdx.x & 63, w = rfl(threadIdx.x >> 6), blk = blockIdx.x;
  const unsigned g = lane >> 4, q4 = (lane >> 2) & 3, p4 = lane & 3, l15 = lane & 15;
  const v4u sent4 = {SENT, SENT, SENT, SENT};
  const f32x4 z4 = {0, 0, 0, 0};
  const v4u ones = {0x3F803F80u, 0x3F803F80u, 0x3F803F80u, 0x3F803F80u};
  const unsigned tr256 = (8 * g + q4) * 256 + (((2 * w + (p4 >> 1)) ^ ((q4 | ((g & 1) << 2)) << 1)) * 16) + 8 * (p4 & 1);
  const unsigned sw256 = (l15 ^ ((g | (((w >> 1) & 1) << 2)) << 1)) * 16;
  const unsigned xc = blk & 7, yc = (blk >> 3) & 15;
  const bool roleA = blk >= 128;
  unsigned tlo, thi, tg;
  {
    unsigned pin0 = tr256, pin1 = sw256;
    asm volatile("s_waitcnt lgkmcnt(0)" : "+s"(t_s), "+s"(k0), "+s"(k1), "+s"(k2) : "v"(pin0), "v"(pin1));
  }
#define KPTR(T, v, i) ((T*)(((u64)(v)[2 * (i) + 1] << 32) | (v)[2 * (i)]))
  const u16 *x = KPTR(const u16, k0, 0), *ckv = KPTR(const u16, k0, 1), *wq = KPTR(const u16, k0, 2), *wuk = KPTR(const u16, k0, 3);
  const u16 *wuv = KPTR(const u16, k1, 0), *wo = KPTR(const u16, k1, 1), *w1 = KPTR(const u16, k1, 2), *w2 = KPTR(const u16, k1, 3);
  u16* out = KPTR(u16, k2, 0);
  char* ws = KPTR(char, k2, 1);
  const rsrc_t rws = mk_rsrc(ws, WS_BYTES);
  const unsigned t_start = (unsigned)t_s;

  if (!roleA) {
    constexpr unsigned L_WUK = 0, L_RED = 0, L_X = 0x8000, L_WO = 0x8000, L_WQ = 0x18000, L_WUV = 0x18000, L_QB = 0x22000, L_QA = 0x23000,
                       L_XC = 0x24000, L_PF = 0x27000, L_CX = 0x2000, L_CNT = 0x23800;
    const unsigned h = xc * 4 + (yc >> 2), j = yc & 3;
    const unsigned hq = h ^ 1;
    const bool p5 = yc < 10;
    const rsrc_t rq = mk_rsrc(wq, 640 * 4096 * 2);
    const unsigned qv = g * 8192 + sw256, qs = (160 * j + 4 * w) * 8192 + hq * 256, ql = L_WQ + w * 1024;
    {
      const unsigned n = w * 64 + lane, row = n / 82, ch = n - row * 82;
      dma(mk_rsrc(x, 4 * 640 * 2), ch < 80 ? row * 1280 + ch * 16 : 0x10000u, 0, L_X + w * 1024);
    }
    v2u ev = ld8_sc1_asm(rws, 0, 0);
#pragma unroll
    for (int i = 0; i < 5; ++i) dma(rq, qv, qs + i * 32 * 8192, ql + i * 8192);
    stg4(SENT, rws, WS_QG + lane * 4, hq * 8192 + j * 2048 + w * 256);
    asm volatile("s_waitcnt vmcnt(6)" : "+v"(ev)::"memory");
    GET_TAG(ev)
    raw_barrier();
    v4u xa[5];
#pragma unroll
    for (int i = 0; i < 5; ++i) xa[i] = *lp<v4u>(L_X + q4 * 1312 + ((p4 + j) & 3) * 320 + g * 16 + i * 64);
    float mean, rstd;
    {
      f32x4 cg = z4, cm = z4;
#pragma unroll
      for (int i = 0; i < 5; ++i) {
        cg = mfma16(xa[i], xa[i], cg);
        cm = mfma16(xa[i], ones, cm);
        xa[i] = p4 == 1 ? ones : xa[i];
      }
      float sq = p4 & 1 ? (p4 & 2 ? cg[3] : cg[1]) : (p4 & 2 ? cg[2] : cg[0]);
      sq = quad_sum(q4 == g ? sq : 0.f);
      sq += dpp<ROR4>(sq);
      sq += dpp<ROR8>(sq);
      mean = sum4(cm) * (1.f / 640.f);
      rstd = __builtin_amdgcn_rsqf(fmaxf(sq * (1.f / 640.f) - mean * mean, 0.f) + 1e-5f);
    }
    unsigned qgo = WS_QG + g * 64 + (l15 >> 1) * 8 + ((lane & 1) << 30), twq = L_WQ + tr256;
    asm volatile("" : "+v"(mean), "+v"(rstd), "+v"(xa[0]), "+v"(xa[1]), "+v"(xa[2]), "+v"(xa[3]), "+v"(xa[4]), "+v"(qgo), "+v"(twq));
    {
      const rsrc_t ruk = mk_rsrc(wuk, 512 * 4096 * 2);
      const unsigned so = (128 * j + 16 * w) * 8192 + h * 256, ld = L_WUK + w * 4096;
#pragma unroll
      for (int e = 0; e < 4; ++e) dma(ruk, (4 * e + g) * 8192 + ((l15 ^ (4 * e + g)) * 16), so, ld + e * 1024);
    }
    VMWAIT(5);
    raw_barrier();
    {
      v4u wf[5];
#pragma unroll
      for (int i = 0; i < 5; ++i) wf[i] = tr_frag(twq + i * 8192, 1024);
      __builtin_amdgcn_sched_barrier(0);
      const f32x4 a = mfma16(xa[4], wf[4], mfma16(xa[2], wf[2], mfma16(xa[0], wf[0], z4))), a2 = mfma16(xa[3], wf[3], mfma16(xa[1], wf[1], z4));
      const float r = ((a[0] + a2[0]) - mean * (a[1] + a2[1])) * rstd;
      const float o = dpp<QP_X1>(r);
      stg8(v2u{pk_bf16(r, o), tg}, rws, qgo, hq * 8192 + j * 2048 + w * 256);
    }
    VMWAIT(1);
    const unsigned pvo = (lane >> 5) * 0x20000 + (lane & 31) * 128, pso = ((xc >> 1) * 4096 + (((xc & 1) << 4) | yc) * 4) * 1024;
    v4u uk[4];
#pragma unroll
    for (int k = 0; k < 4; ++k) uk[k] = *lp<v4u>(L_WUK + w * 4096 + l15 * 256 + (((4 * k + g) ^ l15) * 16));
    {
      v4u g0;
      snooze<H1_DELAY>();
      bool fok = w != 7;
      int lag = 0;
#pragma nounroll
      for (unsigned sp = 0; sp < SPIN_CAP; ++sp) {
        asm volatile("" ::: "memory");
        g0 = ldg16(rws, WS_QG + lane * 16, h * 8192 + w * 1024);
        if (!fok) {
          const v4u a = ldg16(rws, WS_FLAGS + lane * 16, 0), b = ldg16(rws, WS_FLAGS + 1024 + lane * 16, 0);
          fok = flags_ok(a, b, tlo, thi);
          lag = max((int)(a.z - t_start), (int)(b.z - t_start));
        }
        if (__all(fok && g0.y == tg && g0.w == tg)) break;
        __builtin_amdgcn_s_sleep(1);
      }
      if (w == 7) *lp<int>(L_CNT + 16) = min(wave_max(lag), LAG_CAP);
      const unsigned col = 4 * q4 + (w >> 1);
      *lp<v2u>(L_QB + col * 256 + (((8 * (w & 1) + 2 * g + (p4 >> 1)) ^ col) * 16) + (p4 & 1) * 8) = v2u{g0.x, g0.z};
      barrier();
      v4u qf[4];
#pragma unroll
      for (int k = 0; k < 4; ++k) qf[k] = *lp<v4u>(L_QB + l15 * 256 + (((4 * k + g) ^ l15) * 16));
      const f32x4 a = mfma16(qf[2], uk[2], mfma16(qf[0], uk[0], z4)) + mfma16(qf[3], uk[3], mfma16(qf[1], uk[1], z4));
      constexpr float SC = 0.08838834764831845f * 1.4426950408889634f;
      const float r = sum4(a) * SC;
      const float o = dpp<QP_X1>(r);
      if ((lane & 1) == 0) *lp<unsigned>(L_QA + g * 256 + (((2 * w + (l15 >> 3)) ^ (h & 15)) * 16) + (l15 & 7) * 2) = san(pk_bf16(r, o));
    }
    barrier();
    if (w == 0) stg16(*lp<v4u>(L_QA + lane * 16), rws, WS_QABS + g * 32768 + l15 * 16, j * 8192 + h * 256);
    if (w - 1 < PF_CKV) l2_pull(mk_rsrc(ckv, 4 * 4096 * 512 * 2), pvo, pso + (32 - 2 * w) * 0x20000, L_PF + w * 256);
    {
      const int gate = (int)rfl(*lp<unsigned>(L_CNT + 16)) + T_L;
      while ((int)(now32() - t_start) < gate) __builtin_amdgcn_s_sleep(2);
    }
    {
      const rsrc_t ruv = mk_rsrc(wuv, 512 * 4096 * 2);
      const unsigned so = (128 * j + w) * 8192 + h * 256, ld = L_WUV + w * 1024;
#pragma unroll
      for (int i = 0; i < 4; ++i) {
        dma(ruv, g * 16 * 8192 + sw256, so + ((i >> 1) * 64 + (i & 1) * 8) * 8192, ld + i * 8192);
      }
    }
    if (w < 5) l2_pull(mk_rsrc(wo, 4096 * 640 * 2), (lane >> 1) * 1280 + (lane & 1) * 128, (xc * 512 + yc * 32) * 1280 + w * 256, L_PF + 0x800 + w * 256);
    if (p5) {
      VMWAIT(0);
      snooze<WO_DELAY>();
      const rsrc_t ro = mk_rsrc(wo, 4096 * 640 * 2);
      const unsigned v0 = (lane >> 3) * 1280 + (((lane & 7) ^ ((g & 1) << 1)) * 16);
      unsigned so = (xc * 512 + w * 64) * 1280 + yc * 128, ld = L_WO + w * 8192;
#pragma unroll 1
      for (int i = 0; i < 4; ++i) {
        dma(ro, v0, so, ld);
        dma(ro, v0 ^ 64, so + 8 * 1280, ld + 1024);
        so += 16 * 1280;
        ld += 2048;
      }
    }
    unsigned xres = ld4_asm(mk_rsrc(x, 4 * 640 * 2), (2 * (w & 1) + (lane >> 5)) * 1280 + (lane & 31) * 4, xc == 0 ? yc * 128 : 0x10000);
    {
      unsigned t = ld4_asm(rws, lane * 2 * OP_STRIDE + 32768, w == 7 ? WS_OP : 0x7F000000u);
      asm volatile("s_waitcnt vmcnt(0)" : "+v"(t), "+v"(xres)::"memory");
    }
    if (w == 0 && lane == 0) *lp<unsigned>(L_CNT) = 0;
    barrier();
    v4u auv[4], aw[4][2];
#pragma unroll
    for (int k = 0; k < 4; ++k) auv[k] = tr_frag(L_WUV + tr256 + k * 8192, 1024);
    if (p5) {
      const unsigned base = L_WO + w * 8192 + (8 * g + q4) * 128 + (((q4 >> 1) | ((g & 1) << 1)) * 32) + p4 * 8;
#pragma unroll
      for (int n = 0; n < 4; ++n)
#pragma unroll
        for (int k = 0; k < 2; ++k) aw[n][k] = tr_frag((base ^ (n << 5)) + k * 4096, 512);
    }
    {
      v4u st;
      const unsigned so = WS_STATS + (w >> 1) * 32 * 512 + h * 16;
      paced_wait(rws, HINT_B + (128 + l15 * 8 + (w & 6) + (g & 1)) * 128, tg, lane < 32, t_start, D_S);
      st = ldg16(rws, (lane & 31) * 512, so);
#pragma nounroll
      for (unsigned sp = 0; sp < SPIN_CAP; ++sp) {
        const bool ok = (st.y == tg && st.w == tg) || lane >= 32;
        if (__all(ok)) break;
        asm volatile("" ::: "memory");
        __builtin_amdgcn_s_sleep(1);
        if (!ok) st = ldg16(rws, (lane & 31) * 512, so);
      }
      asm volatile("" ::: "memory");
      v4u o[4];
      const unsigned vo = g * OP_STRIDE + l15 * 16, so2 = WS_OP + 16 * w * OP_STRIDE + h * 1024 + j * 256;
#pragma unroll
      for (int i = 0; i < 4; ++i) o[i] = __builtin_amdgcn_raw_buffer_load_b128(rws, vo, so2 + 4 * i * OP_STRIDE, SC1 | NT);
      __builtin_amdgcn_sched_barrier(0);
      if (lane == 0 && __hip_atomic_fetch_add(lp<unsigned>(L_CNT), 1u, __ATOMIC_RELAXED, __HIP_MEMORY_SCOPE_WORKGROUP) == 7) announce(rws, blk * 128, tg);
      const float m = __uint_as_float(st.x), l = __uint_as_float(st.z);
      float M = max2(m, dpp<QP_X1>(m));
      M = max2(M, dpp<QP_X2>(M));
      M = max2(M, dpp<ROR4>(M));
      M = max2(M, dpp<ROR8>(M));
      {
        auto r = __builtin_amdgcn_permlane16_swap(__float_as_uint(M), __float_as_uint(M), false, false);
        M = max2(__uint_as_float(r[0]), __uint_as_float(r[1]));
      }
      const float wg = __builtin_amdgcn_exp2f(m - M);
      float L = quad_sum(l * wg);
      L += dpp<ROR4>(L);
      L += dpp<ROR8>(L);
      L = sum16(L);
      const float co = wg * __builtin_amdgcn_rcpf(L);
      float cf[4];
#pragma unroll
      for (int i = 0; i < 4; ++i) {
        cf[i] = __int_as_float(__builtin_amdgcn_ds_bpermute((16 * (w & 1) + 4 * i + g) * 4, __float_as_int(co)));
        PIN4(cf[i]);
      }
      f32x2 t[4] = {};
#pragma unroll
      for (int i = 0; i < 4; ++i)
#pragma unroll
        for (int e = 0; e < 4; ++e) t[e] += cf[i] * f32x2{bf_lo(o[i][e]), bf_hi(o[i][e])};
      *lp<v4u>(L_XC + w * 1024 + g * 256 + ((l15 ^ (4 * (w >> 1) + g)) * 16)) =
          v4u{pk_bf16(t[0].x, t[0].y), pk_bf16(t[1].x, t[1].y), pk_bf16(t[2].x, t[2].y), pk_bf16(t[3].x, t[3].y)};
    }
    barrier();
    const unsigned h4o = (((w >> 1) * 4 + p4) * 8 + xc) * 128;
    v2u h4 = ld8_sc1_asm(rws, WS_HINT + h4o, 0);
    {
      f32x4 a0 = z4, a1 = z4;
      const unsigned rb = L_XC + q4 * 2048 + p4 * 256;
      v4u xf[8];
#pragma unroll
      for (int k = 0; k < 4; ++k) {
        const unsigned co = ((4 * k + g) ^ l15) * 16;
        xf[2 * k] = *lp<v4u>(rb + co);
        xf[2 * k + 1] = *lp<v4u>(rb + 1024 + co);
      }
      __builtin_amdgcn_sched_barrier(0);
#pragma unroll
      for (int k = 0; k < 4; ++k) {
        a0 = mfma16(xf[2 * k], auv[k], a0);
        a1 = mfma16(xf[2 * k + 1], auv[k], a1);
      }
      const float r = sum4(a0 + a1);
      const float o = dpp<QP_X1>(r);
      if ((lane & 1) == 0) stg4(san(pk_bf16(r, o)), rws, WS_CTX + g * 32 + l15 * 2, ((h * 4 + j) * 8 + w) * 128);
    }
    asm volatile("s_waitcnt vmcnt(1)" : "+v"(h4)::"memory");
    if (p5) {
      paced_wait(rws, h4o, tg, lane < 4, t_start, D_4, h4);
      if (lane == 0 && __hip_atomic_fetch_add(lp<unsigned>(L_CNT), 1u, __ATOMIC_RELAXED, __HIP_MEMORY_SCOPE_WORKGROUP) == 15) announce(rws, HINT_B + blk * 128, tg);
      const unsigned vo = WS_CTX + (lane >> 5) * 1024 + (((lane & 31) ^ (lane >> 5)) * 16), so = (4 * xc + (w >> 1)) * 4096 + (w & 1) * 512;
      const unsigned cl = L_CX + w * 2048, cr = cl + p4 * 512 + ((((g >> 1) * 8 + q4 * 2 + (g & 1)) ^ ((p4 & 1) | ((p4 >> 1) << 3))) * 16);
      v4u c0, c1;
      bool n0 = true, n1 = true;
#pragma nounroll
      for (unsigned sp = 0; sp < SPIN_CAP; ++sp) {
        if (n0) dma_sc1(rws, vo, so, cl);
        if (n1) dma_sc1(rws, vo ^ 128, so + 2048, cl + 1024);
        VMWAIT(0);
        c0 = *lp<v4u>(cr), c1 = *lp<v4u>(cr + 256);
        const u64 bad = __ballot(max(umax4(c0), umax4(c1)) == SENT);
        if (bad == 0) break;
        n0 = (bad & 0x3333333333333333ull) != 0, n1 = (bad & 0xCCCCCCCCCCCCCCCCull) != 0;
      }
      f32x4 a[4];
#pragma unroll
      for (int n = 0; n < 4; ++n) a[n] = mfma16(c0, aw[n][0], z4);
#pragma unroll
      for (int n = 0; n < 4; ++n) a[n] = mfma16(c1, aw[n][1], a[n]);
#pragma unroll
      for (int n = 0; n < 4; ++n) *lp<float>(L_RED + w * 1024 + g * 256 + n * 64 + l15 * 4) = sum4(a[n]);
      barrier();
      if (w < 2) {
        f32x2 t[8];
#pragma unroll
        for (int i = 0; i < 8; ++i) t[i] = *lp<f32x2>(L_RED + i * 1024 + w * 512 + lane * 8);
        const f32x2 s = ((t[0] + t[1]) + (t[2] + t[3])) + ((t[4] + t[5]) + (t[6] + t[7])) + f32x2{bf_lo(xres), bf_hi(xres)};
        stg4(san(pk_bf16(s.x, s.y)), rws, WS_HP + (g & 1) * 1024 + (lane >> 5) * 512 + l15 * 4, w * 20480 + yc * 2048 + xc * 64);
      }
    } else if (yc < 15 && w < 2) {
    const unsigned nt = (yc - 10) * 8 + xc, bp = w;
    const unsigned grp = lane >> 3, sub = lane & 7;
    v4u f[7];
    v2u hp;
    const unsigned fo = WS_FP + grp * 256 + sub * 16, fs = nt * 54 * 256 + bp * 128;
    const unsigned f6 = WS_FP + (grp < 6 ? 48 + grp : 53) * 256 + sub * 16;
    const unsigned ho = WS_HP + (8 * (sub >> 2) + grp) * 64 + (sub & 3) * 8, hs = bp * 20480 + (nt >> 1) * 1024 + (nt & 1) * 32;
    {
      const unsigned k = lane >> 1;
      const unsigned ho6 = (128 + (4 * (k >> 3) + 2 * (lane & 1) + bp) * 8 + (k & 7)) * 128;
      wait_hint(rws, WS_HINT + 4 + ho6, 0, tg, lane < 54);
    }
    snooze<H6_DELAY>();
    hp = ldg8(rws, ho, hs);
#pragma unroll
    for (int i = 0; i < 6; ++i) f[i] = ldg16(rws, fo + i * 2048, fs);
    f[6] = ldg16(rws, f6, fs);
#pragma nounroll
    for (unsigned sp = 0; sp < SPIN_CAP; ++sp) {
      unsigned mx[7], m = max(hp.x, hp.y);
      const bool bh = m == SENT;
#pragma unroll
      for (int i = 0; i < 7; ++i) mx[i] = umax4(f[i]), m = max(m, mx[i]);
      if (__all(m != SENT)) break;
      asm volatile("" ::: "memory");
      if (bh) hp = ldg8(rws, ho, hs);
#pragma unroll
      for (int i = 0; i < 6; ++i)
        if (mx[i] == SENT) f[i] = ldg16(rws, fo + i * 2048, fs);
      if (mx[6] == SENT) f[6] = ldg16(rws, f6, fs);
    }
    f32x4 s4 = f32x4{bf_lo(hp.x), bf_hi(hp.x), bf_lo(hp.y), bf_hi(hp.y)};
    if (grp < 6) s4 += __builtin_bit_cast(f32x4, f[6]);
    s4 += ((__builtin_bit_cast(f32x4, f[0]) + __builtin_bit_cast(f32x4, f[1])) + (__builtin_bit_cast(f32x4, f[2]) + __builtin_bit_cast(f32x4, f[3]))) +
          (__builtin_bit_cast(f32x4, f[4]) + __builtin_bit_cast(f32x4, f[5]));
#pragma unroll
    for (int e = 0; e < 4; ++e) s4[e] = sum32(sum16(s4[e] + dpp<ROR8>(s4[e])));
    if (lane < 8)
      stg8(v2u{pk_bf16(s4[0], s4[1]), pk_bf16(s4[2], s4[3])}, mk_rsrc(out, 4 * 640 * 2), (sub >> 2) * 1280 + (sub & 3) * 8, bp * 2560 + nt * 32);
    if (nt == 0 && bp == 0 && lane == 0) stg8(v2u{tlo, thi}, rws, WS_EPOCH, 0);
  }
  } else {
    constexpr unsigned L_QAB = 0x20000, L_RED = 0, L_PB = 0x4000, L_MX = 0x6000, L_LS = 0x6800, L_W2 = 0x8000, L_W1 = 0x12000,
                       L_CS = 0x1D000, L_ST = 0x1D400, L_HB = 0x20000;
    const unsigned b = xc >> 1, s = ((xc & 1) << 4) | yc, p = b * 32 + s;
    const unsigned kf = xc + 8 * (yc >> 2), fcu = 2 * kf + ((yc >> 1) & 1), be = yc & 1;
    const bool ffn = kf < 27;
    const unsigned l31 = lane & 31, hi = lane >> 5;
    v2u ev = ld8_sc1_asm(rws, 0, 0);
    {
      const unsigned a = blk - 128;
      unsigned vo = 0x7F000000u;
      if (w == 7) vo = WS_QABS + a * 1024 + lane * 16;
      if (w == 6) vo = WS_CTX + a * 1024 + lane * 16;
      if (w == 5 && a < 40) vo = WS_HP + a * 1024 + lane * 16;
      if (w == 0 && lane < 32) vo = WS_STATS + p * 512 + lane * 16;
      if (w == 0 && lane >= 48) vo = WS_HINT + ((lane >> 3) & 1) * HINT_B + blk * 128 + (lane & 7) * 16;
      if (w == 4 && lane < 16) vo = WS_HINT + (lane >> 3) * HINT_B + a * 128 + (lane & 7) * 16;
      stg16(sent4, rws, vo, 0);
      if (ffn && lane < 40) stg16(sent4, rws, WS_FP + (lane >> 3) * (8 * 54 * 256) + (lane & 7) * 16, (w * 54 + fcu) * 256 + be * 128);
    }
    asm volatile("s_waitcnt vmcnt(0)" : "+v"(ev)::"memory");
    GET_TAG(ev)
    raw_barrier();
    if (w == 0 && lane == 0) stg16(v4u{tlo, thi, t_start, 0}, rws, WS_FLAGS, (blk - 128) * 16);
    if (w == 3) l2_pull(rws, 32768, WS_OP + p * OP_STRIDE, 0x26000);
    snooze<A_DELAY>();
    const rsrc_t rc = mk_rsrc(ckv, 4 * 4096 * 512 * 2);
    const unsigned cso = (b * 4096 + (w * 32 + s) * 4) * 1024, cld = w * 4096;
    const unsigned qso = WS_QABS + b * 32768 + w * 4096, qld = L_QAB + w * 4096;
    unsigned ve[4];
#pragma unroll
    for (int e = 0; e < 4; ++e) ve[e] = (lane ^ swz((4 * w + e) & 15)) * 16;
#pragma unroll 1
    for (unsigned kt = 0; kt < 2; ++kt) {
#pragma unroll
      for (int e = 0; e < 4; ++e) dma(rc, ve[e], cso + kt * 0x100000 + e * 1024, cld + kt * 32768 + e * 1024);
    }
    v4u fa = {0, 0, 0, 0}, fb = {0, 0, 0, 0};
    asm volatile(
        "s_cmp_eq_u32 %4, 7\n\ts_cbranch_scc0 1f\n\tbuffer_load_dwordx4 %0, %2, %3, 0 offen offset:1024 sc1\n\tbuffer_load_dwordx4 %1, %2, %3, 0 offen offset:2048 sc1\n1:"
        : "+v"(fa), "+v"(fb)
        : "v"(lane * 16), "s"(rws), "s"(w)
        : "scc", "memory");
#pragma unroll
    for (int e = 0; e < 4; ++e) dma(rc, ve[e], cso + 0x200000 + e * 1024, cld + 65536 + e * 1024);
    asm volatile("s_cmp_eq_u32 %2, 7\n\ts_cbranch_scc0 1f\n\ts_waitcnt vmcnt(4)\n1:" : "+v"(fa), "+v"(fb) : "s"(w) : "scc", "memory");
    if (w == 7) {
#pragma nounroll
      for (unsigned sp = 0; sp < SPIN_CAP && !__all(flags_ok(fa, fb, tlo, thi)); ++sp) {
        __builtin_amdgcn_s_sleep(8);
        asm volatile("" ::: "memory");
        fa = ldg16(rws, WS_FLAGS + lane * 16, 0), fb = ldg16(rws, WS_FLAGS + 1024 + lane * 16, 0);
      }
      *lp<int>(L_QAB + 0x7F00) = min(wave_max(max((int)(fa.z - t_start), (int)(fb.z - t_start))), LAG_CAP);
    }
    barrier();
    const int q_gate = (int)rfl(*lp<unsigned>(L_QAB + 0x7F00)) + T_Q;
    raw_barrier();
    {
      while ((int)(now32() - t_start) < q_gate) __builtin_amdgcn_s_sleep(1);
#pragma unroll
      for (int i = 0; i < 4; ++i) dma_sc1(rws, lane * 16, qso + i * 1024, qld + i * 1024);
#pragma unroll
      for (int e = 0; e < 4; ++e) dma(rc, ve[e], cso + 0x300000 + e * 1024, cld + 98304 + e * 1024);
    }
    VMWAIT(8);
    raw_barrier();
    v4u ka[16], vb[4][4];
    const unsigned kb = (16 * w + l15) * 1024 + ((g ^ swz(l15)) << 4);
    const unsigned va = (4 * g + q4) * 1024 + (w >> 1) * 256 + (((8 * (w & 1) + (p4 >> 1)) ^ swz(4 * g + q4)) << 4) + 8 * (p4 & 1);
    if (w < 6) {
#pragma unroll
      for (int t = 0; t < 16; ++t) ka[t] = *lp<v4u>((kb ^ ((t & 3) << 6)) + (t >> 2) * 256);
    }
#pragma unroll
    for (int sg = 0; sg < 3; ++sg)
#pragma unroll
      for (int t = 0; t < 4; ++t) vb[t][sg] = tr_frag(((sg < 2 ? va : va + 65536) ^ (t << 5)) + (sg & 1) * 32768, 16384);
    VMWAIT(4);
#pragma nounroll
    for (unsigned sp = 0; sp < SPIN_CAP; ++sp) {
      unsigned m = 0;
#pragma unroll
      for (int i = 0; i < 4; ++i) m = max(m, umax4(*lp<v4u>(qld + i * 1024 + lane * 16)));
      if (__all(m != SENT)) break;
#pragma unroll
      for (int i = 0; i < 4; ++i) dma_sc1(rws, lane * 16, qso + i * 1024, qld + i * 1024);
      VMWAIT(0);
    }
    VMWAIT(0);
    barrier();
    const unsigned pu = yc * 3 + w, puk = pu / 10, pur = pu - puk * 10;
    const bool pul = w < 3 && puk < (xc < 3 ? 4u : 3u);
    if (pul) l2_pull(mk_rsrc(w1, 640 * 1728 * 2), lane * 3456, pur * 64 * 3456 + (xc + 8 * puk) * 128, 0x7000 + w * 256);
    if (w >= 6) {
#pragma unroll
      for (int t = 0; t < 16; ++t) ka[t] = *lp<v4u>((kb ^ ((t & 3) << 6)) + (t >> 2) * 256);
    }
    float sc[8], M[2];
    {
      const unsigned base = L_QAB + l15 * 256 + ((g ^ l15) << 4);
      f32x4 a0 = z4, a1 = z4;
      v4u qa[16], qb[16];
#pragma unroll
      for (int t = 0; t < 8; ++t) {
        qa[t] = *lp<v4u>((base ^ ((t & 3) << 6)) + (t >> 2) * 8192);
        qb[t] = *lp<v4u>((base ^ ((t & 3) << 6)) + (t >> 2) * 8192 + 4096);
      }
      __builtin_amdgcn_sched_barrier(0);
#pragma unroll
      for (int t = 0; t < 16; ++t) {
        if (t < 8) {
          qa[t + 8] = *lp<v4u>((base ^ ((t & 3) << 6)) + ((t >> 2) + 2) * 8192);
          qb[t + 8] = *lp<v4u>((base ^ ((t & 3) << 6)) + ((t >> 2) + 2) * 8192 + 4096);
        } else if (t < 12) {
          vb[t - 8][3] = tr_frag(((va + 65536) ^ ((t - 8) << 5)) + 32768, 16384);
        }
        a0 = mfma16(ka[t], qa[t], a0);
        a1 = mfma16(ka[t], qb[t], a1);
        __builtin_amdgcn_sched_barrier(0);
      }
#pragma unroll
      for (int e = 0; e < 4; ++e) sc[e] = a0[e], sc[4 + e] = a1[e];
    }
    {
      const float m0 = max4(sc[0], sc[1], sc[2], sc[3]), m1 = max4(sc[4], sc[5], sc[6], sc[7]);
      auto r = __builtin_amdgcn_permlane16_swap(__float_as_uint(m0), __float_as_uint(m1), false, false);
      const float m = max2(__uint_as_float(r[0]), __uint_as_float(r[1]));
      auto r2 = __builtin_amdgcn_permlane32_swap(__float_as_uint(m), __float_as_uint(m), false, false);
      if (hi == 0) *lp<float>(L_MX + l31 * 48 + w * 4) = max2(__uint_as_float(r2[0]), __uint_as_float(r2[1]));
    }
    barrier();
    {
#pragma unroll
      for (int u = 0; u < 2; ++u) {
        const f32x4 m0 = *lp<f32x4>(L_MX + (l15 + 16 * u) * 48), m1 = *lp<f32x4>(L_MX + (l15 + 16 * u) * 48 + 16);
        M[u] = max2(max4(m0[0], m0[1], m0[2], m0[3]), max4(m1[0], m1[1], m1[2], m1[3]));
      }
#pragma unroll
      for (int e = 0; e < 8; ++e) sc[e] = __builtin_amdgcn_exp2f(sc[e] - M[e >> 2]);
      const unsigned po = L_PB + (w >> 1) * 1024 + lane * 16 + (w & 1) * 8;
      *lp<v2u>(po) = v2u{pk_bf16(sc[0], sc[1]), pk_bf16(sc[2], sc[3])};
      *lp<v2u>(po + 4096) = v2u{pk_bf16(sc[4], sc[5]), pk_bf16(sc[6], sc[7])};
      barrier();
      if (w == 0 && lane == 0) announce(rws, HINT_B + blk * 128, tg);
    }
    {
      v4u pf[8];
#pragma unroll
      for (int sg = 0; sg < 8; ++sg) pf[sg] = *lp<v4u>(L_PB + sg * 1024 + lane * 16);
      __builtin_amdgcn_sched_barrier(0);
      const unsigned vo = g * 4096 + l15 * 8, so = WS_OP + p * OP_STRIDE + w * 128;
      f32x4 o[2][4];
#pragma unroll
      for (int u = 0; u < 2; ++u)
#pragma unroll
        for (int sg = 0; sg < 4; ++sg)
#pragma unroll
          for (int t = 0; t < 4; ++t) o[u][t] = mfma16(pf[4 * u + sg], vb[t][sg], sg ? o[u][t] : z4);
#pragma unroll
      for (int u = 0; u < 2; ++u)
#pragma unroll
        for (int r = 0; r < 4; ++r)
          stg8(v2u{pk_bf16(o[u][0][r], o[u][1][r]), pk_bf16(o[u][2][r], o[u][3][r])}, rws, vo + r * 1024, so + u * 16384);
      const float l = sum32(fold16((sc[0] + sc[1]) + (sc[2] + sc[3]), (sc[4] + sc[5]) + (sc[6] + sc[7])));
      if (hi == 0) *lp<float>(L_LS + l31 * 48 + w * 4) = l;
    }
    VMWAIT(0);
    barrier();
    if (w == 0) {
      const float L = sum4(*lp<f32x4>(L_LS + l31 * 48)) + sum4(*lp<f32x4>(L_LS + l31 * 48 + 16));
      if (hi == 0) stg16(v4u{__float_as_uint(g & 1 ? M[1] : M[0]), tg, __float_as_uint(L), tg}, rws, WS_STATS + l31 * 16, p * 512);
    }
    raw_barrier();
    const unsigned t_pub = now32();
    const unsigned k0 = 4 * (w >> 2) + (w & 3);
    if (ffn) {
      const rsrc_t r1 = mk_rsrc(w1, 640 * 1728 * 2);
      const unsigned voff = (lane >> 2) * 3456 + ((p4 ^ (hi << 1)) * 16);
      unsigned so = k0 * 32 * 3456 + fcu * 64, ld = L_W1 + k0 * 2048;
      snooze<F_DELAY>();
#pragma unroll 1
      for (unsigned i = w >> 2; i < 3; ++i) {
        dma(r1, voff, so, ld);
        dma(r1, voff, so + 16 * 3456, ld + 1024);
        so += 256 * 3456;
        ld += 16384;
      }
    }
    while (now32() - t_pub < D_F) __builtin_amdgcn_s_sleep(2);
    if (pul) l2_pull(mk_rsrc(w2, 1728 * 640 * 2), lane * 128, (xc + 8 * puk) * 64 * 1280 + pur * 8192, 0x7000 + w * 256);
    VMWAIT(0);
    raw_barrier();
    snooze<WO_DELAY>();
    if (ffn) {
      {
        const rsrc_t r2 = mk_rsrc(w2, 1728 * 640 * 2);
        const unsigned s2 = (32 * fcu + 4 * w) * 1280, l2 = L_W2 + w * 1024;
#pragma unroll 1
        for (unsigned i = 0; i < 5; ++i) dma(r2, g * 1280 + sw256, s2 + i * 256, l2 + i * 8192);
      }
      VMWAIT(0);
      raw_barrier();
      const unsigned t1 = L_W1 + k0 * 2048 + (8 * g + q4) * 64 + (g & 1) * 32 + p4 * 8;
      v4u a10[3], a11[3], a2[5];
      const v4u zero4 = {0, 0, 0, 0};
#pragma unroll
      for (int i = 0; i < 3; ++i) {
        if (i < 2 || w < 4) {
          a10[i] = tr_frag(t1 + i * 16384, 256);
          a11[i] = tr_frag((t1 ^ 32) + i * 16384, 256);
        } else {
          a10[i] = zero4, a11[i] = zero4;
        }
      }
#pragma unroll
      for (int i = 0; i < 5; ++i) a2[i] = tr_frag(L_W2 + tr256 + i * 8192, 1024);
      f32x2 cs;
      {
        f32x4 c0 = z4, c1 = z4;
#pragma unroll
        for (int i = 0; i < 3; ++i) {
          c0 = mfma16(ones, a10[i], c0);
          c1 = mfma16(ones, a11[i], c1);
        }
        if (g == 0) {
          *lp<float>(L_CS + w * 128 + l15 * 4) = c0[0];
          *lp<float>(L_CS + w * 128 + 64 + l15 * 4) = c1[0];
        }
        barrier();
        cs = f32x2{0, 0};
#pragma unroll
        for (int i = 0; i < 8; ++i) cs += *lp<f32x2>(L_CS + i * 128 + g * 32 + p4 * 8);
        PIN4(cs);
      }
      const unsigned hv = WS_HP + (lane ^ ((0u - (lane >> 4)) & 3)) * 16, hs = be * 20480 + k0 * 1024;
      const unsigned hl = L_HB + w * 3072;
      unsigned hr = hl + l15 * 64 + ((g ^ ((0u - q4) & 3)) * 16);
      unsigned ro = L_RED + ((g >> 1) * 16 + (g & 1) * 8 + (l15 >> 1)) * 64 + w * 8 + (l15 & 1) * 4, sto = L_ST + (g >> 1) * 64 + (g & 1) * 32 + w * 4;
      unsigned rb = L_RED + ((l15 >> 3) * 16 + g * 4 + p4) * 64 + (p4 << 4), stb = L_ST + (l15 >> 3) * 64 + p4 * 16;
      unsigned fpo = WS_FP + (lane >> 5) * 64 + l15 * 4 + (g & 1) * (8 * 54 * 256);
      v4u on = ones;
      asm volatile("" : "+v"(hr), "+v"(ro), "+v"(sto), "+v"(rb), "+v"(stb), "+v"(fpo), "+v"(on));
      if (w >= 4) *lp<v4u>(hl + 2048 + lane * 16) = zero4;
      if (w < 2) paced_wait(rws, HINT_B + (w * 40 + lane) * 128, tg, lane < 40, t_start, D_5);
      raw_barrier();
      v4u hb[3];
      bool n0 = true, n1 = true, n2 = w < 4;
#pragma nounroll
      for (unsigned sp = 0; sp < SPIN_CAP; ++sp) {
        if (n0) dma_sc1(rws, hv, hs, hl);
        if (n1) dma_sc1(rws, hv, hs + 8192, hl + 1024);
        if (n2) dma_sc1(rws, hv, hs + 16384, hl + 2048);
        VMWAIT(0);
#pragma unroll
        for (int i = 0; i < 3; ++i) hb[i] = *lp<v4u>(hr + i * 1024);
        n0 = __any(umax4(hb[0]) == SENT), n1 = __any(umax4(hb[1]) == SENT), n2 = __any(umax4(hb[2]) == SENT);
        if (!(n0 || n1 || n2)) break;
      }
      if (w == 0 && lane == 0) announce(rws, blk * 128, tg);
      {
        f32x4 c0 = z4, c1 = z4, cm = z4, cg = z4;
#pragma unroll
        for (int i = 0; i < 3; ++i) {
          cg = mfma16(hb[i], hb[i], cg);
          cm = mfma16(hb[i], on, cm);
          c0 = mfma16(hb[i], a10[i], c0);
          c1 = mfma16(hb[i], a11[i], c1);
        }
        float sq = quad_sum((l15 >> 3) == (g >> 1) ? sum4(cg) : 0.f);
        sq += dpp<ROR4>(sq);
        sq += dpp<ROR8>(sq);
        const float sv = fold16(sum4(cm), sq);
        if (l15 == 0) *lp<float>(sto) = sv;
        *lp<float>(ro) = fold16(sum4(c0), sum4(c1));
      }
      barrier();
      {
        const f32x4 st = *lp<f32x4>(stb);
        const f32x4 t4 = (*lp<f32x4>(rb) + *lp<f32x4>(rb ^ 16)) + (*lp<f32x4>(rb ^ 32) + *lp<f32x4>(rb ^ 48));
        const f32x2 t = {t4[0] + t4[2], t4[1] + t4[3]};
        float sx = sum4(st);
        sx += dpp<QP_X1>(sx);
        const float mean = dpp<0x00>(sx) * (1.f / 640.f);
        const float var = fmaxf(dpp<0xAA>(sx) * (1.f / 640.f) - mean * mean, 0.f);
        const float rstd = __builtin_amdgcn_rsqf(var + 1e-5f);
        const float u0 = (t.x - mean * cs.x) * rstd, u1 = (t.y - mean * cs.y) * rstd;
        const float y0 = u0 * __builtin_amdgcn_rcpf(1.f + __builtin_amdgcn_exp2f(-1.4426950408889634f * u0));
        const float y1 = u1 * __builtin_amdgcn_rcpf(1.f + __builtin_amdgcn_exp2f(-1.4426950408889634f * u1));
        const unsigned y = pk_bf16(y0, y1);
        const v4u ah = {dppu<0x00>(y), dppu<0x55>(y), dppu<0xAA>(y), dppu<0xFF>(y)};
        f32x4 dd[5];
#pragma unroll
        for (int i = 0; i < 5; ++i) dd[i] = mfma16(ah, a2[i], z4);
        __builtin_amdgcn_sched_barrier(0);
        const unsigned fs = (w * 54 + fcu) * 256 + be * 128;
        const bool od = g & 1;
        stg4(san(__float_as_uint(od ? dd[1][0] : dd[0][0])), rws, fpo, fs);
        stg4(san(__float_as_uint(od ? dd[3][0] : dd[2][0])), rws, fpo, fs + 16 * 54 * 256);
        if (!od) stg4(san(__float_as_uint(dd[4][0])), rws, fpo, fs + 32 * 54 * 256);
      }
    }
  }
  asm volatile(".Lfused_pf_end:");
}
}

extern "C" void kernel_launch(void* const* d_in, const int* in_sizes, int n_in, void* d_out, int out_size, void* d_ws, size_t ws_size,
                              hipStream_t stream) {
  (void)in_sizes, (void)n_in, (void)out_size, (void)ws_size;
  (void)hipFuncSetAttribute((const void*)fused_kernel, hipFuncAttributeMaxDynamicSharedMemorySize, LDS_BYTES);
  fused_kernel<<<256, 512, LDS_BYTES, stream>>>((const u16*)d_in[0], (const u16*)d_in[1], (const u16*)d_in[2], (const u16*)d_in[3],
                                                (const u16*)d_in[4], (const u16*)d_in[5], (const u16*)d_in[7], (const u16*)d_in[8],
                                                (u16*)d_out, (char*)d_ws);
}
```

```cpp
#include <hip/hip_runtime.h>
#include <stdint.h>

namespace {
typedef unsigned short u16;
typedef unsigned long long u64;
typedef unsigned v4u __attribute__((ext_vector_type(4)));
typedef unsigned v8u __attribute__((ext_vector_type(8)));
typedef unsigned v2u __attribute__((ext_vector_type(2)));
typedef float f32x2 __attribute__((ext_vector_type(2)));
typedef float f32x4 __attribute__((ext_vector_type(4)));
typedef __bf16 bf16x2 __attribute__((ext_vector_type(2)));
typedef __bf16 bf16x8 __attribute__((ext_vector_type(8)));
typedef short s16x4 __attribute__((ext_vector_type(4)));
#define LAS __attribute__((address_space(3)))
#define GAS __attribute__((address_space(1)))
typedef __amdgpu_buffer_rsrc_t rsrc_t;
#define DEVI __device__ __forceinline__

constexpr unsigned WS_EPOCH = 0;
constexpr unsigned WS_FLAGS = 0x4000;
constexpr unsigned WS_QG = 0x100000 + 0x20000;
constexpr unsigned WS_QABS = 0x20000;
constexpr unsigned WS_STATS = 0x170000;
constexpr unsigned WS_HP = 0x48000;
constexpr unsigned WS_CTX = 0x60000;
constexpr unsigned WS_FP = 0x80000;
constexpr unsigned WS_HINT = 0x190000;
constexpr unsigned WS_OP = 0x200000;
constexpr unsigned OP_STRIDE = 32768 + 128;
constexpr unsigned WS_BYTES = WS_OP + 128 * OP_STRIDE;
constexpr unsigned SENT = 0xFFFFFFFFu;
constexpr unsigned SPIN_CAP = 1u << 15;
constexpr int SC1 = 16, NT = 2;
constexpr unsigned HINT_B = 0x8000;
constexpr int LDS_BYTES = 160 * 1024;

DEVI rsrc_t mk_rsrc(const void* p, unsigned bytes) { return __builtin_amdgcn_make_buffer_rsrc((void*)p, (short)0, (int)bytes, 0x00020000); }
DEVI unsigned rfl(unsigned v) { return __builtin_amdgcn_readfirstlane(v); }
template <typename T> DEVI LAS T* lp(unsigned off) { return (LAS T*)(uintptr_t)off; }
DEVI void barrier() {
  asm volatile("s_waitcnt lgkmcnt(0)" ::: "memory");
  __builtin_amdgcn_s_barrier();
  asm volatile("" ::: "memory");
}
#define VMWAIT(n) asm volatile("s_waitcnt vmcnt(" #n ")" ::: "memory")

DEVI void dma(rsrc_t rs, unsigned voff, unsigned soff, unsigned lds) {
  asm volatile("s_mov_b32 m0, %0\n\ts_nop 0\n\tbuffer_load_dwordx4 %1, %2, %3 offen lds" ::"s"(lds), "v"(voff), "s"(rs), "s"(soff) : "memory");
}
DEVI void dma_nt(rsrc_t rs, unsigned voff, unsigned soff, unsigned lds) {
  asm volatile("s_mov_b32 m0, %0\n\ts_nop 0\n\tbuffer_load_dwordx4 %1, %2, %3 offen nt lds" ::"s"(lds), "v"(voff), "s"(rs), "s"(soff) : "memory");
}
DEVI void dma_sc1(rsrc_t rs, unsigned voff, unsigned soff, unsigned lds) {
  asm volatile("s_mov_b32 m0, %0\n\ts_nop 0\n\tbuffer_load_dwordx4 %1, %2, %3 offen sc1 lds" ::"s"(lds), "v"(voff), "s"(rs), "s"(soff) : "memory");
}
DEVI v2u ld8_sc1_asm(rsrc_t rs, unsigned voff, unsigned soff) {
  v2u r;
  asm volatile("buffer_load_dwordx2 %0, %1, %2, %3 offen sc1" : "=&v"(r) : "v"(voff), "s"(rs), "s"(soff) : "memory");
  return r;
}
DEVI unsigned ld4_asm(rsrc_t rs, unsigned voff, unsigned soff) {
  unsigned r;
  asm volatile("buffer_load_dword %0, %1, %2, %3 offen" : "=&v"(r) : "v"(voff), "s"(rs), "s"(soff) : "memory");
  return r;
}
DEVI v4u ldg16(rsrc_t rs, unsigned voff, unsigned soff) { return __builtin_amdgcn_raw_buffer_load_b128(rs, voff, soff, SC1); }
DEVI v2u ldg8(rsrc_t rs, unsigned voff, unsigned soff) { return __builtin_amdgcn_raw_buffer_load_b64(rs, voff, soff, SC1); }
DEVI void stg16(v4u v, rsrc_t rs, unsigned voff, unsigned soff) { __builtin_amdgcn_raw_buffer_store_b128(v, rs, voff, soff, SC1); }
DEVI void stg8(v2u v, rsrc_t rs, unsigned voff, unsigned soff) { __builtin_amdgcn_raw_buffer_store_b64(v, rs, voff, soff, SC1); }
DEVI void stg4(unsigned v, rsrc_t rs, unsigned voff, unsigned soff) { __builtin_amdgcn_raw_buffer_store_b32(v, rs, voff, soff, SC1); }

DEVI unsigned pk_bf16(float a, float b) {
  f32x2 f = {a, b};
  return __builtin_bit_cast(unsigned, __builtin_convertvector(f, bf16x2));
}
DEVI float bf_lo(unsigned u) { return __uint_as_float(u << 16); }
DEVI float bf_hi(unsigned u) { return __uint_as_float(u & 0xFFFF0000u); }
DEVI unsigned san(unsigned u) { return u < 0xFFFFFFFEu ? u : 0xFFFFFFFEu; }
DEVI unsigned umax4(v4u a) { return max(max(a.x, a.y), max(a.z, a.w)); }
DEVI f32x4 mfma16(v4u a, v4u b, f32x4 c) {
  return __builtin_amdgcn_mfma_f32_16x16x32_bf16(__builtin_bit_cast(bf16x8, a), __builtin_bit_cast(bf16x8, b), c, 0, 0, 0);
}
template <int CTRL> DEVI float dpp(float v) {
  return __int_as_float(__builtin_amdgcn_update_dpp(0, __float_as_int(v), CTRL, 0xF, 0xF, true));
}
template <int CTRL> DEVI unsigned dppu(unsigned v) { return (unsigned)__builtin_amdgcn_update_dpp(0, (int)v, CTRL, 0xF, 0xF, true); }
constexpr int QP_X1 = 0xB1, QP_X2 = 0x4E;
constexpr int ROR4 = 0x124, ROR8 = 0x128;
DEVI float quad_sum(float v) {
  v += dpp<QP_X1>(v);
  v += dpp<QP_X2>(v);
  return v;
}
DEVI float sum16(float v) {
  auto r = __builtin_amdgcn_permlane16_swap(__float_as_uint(v), __float_as_uint(v), false, false);
  return __uint_as_float(r[0]) + __uint_as_float(r[1]);
}
DEVI float sum32(float v) {
  auto r = __builtin_amdgcn_permlane32_swap(__float_as_uint(v), __float_as_uint(v), false, false);
  return __uint_as_float(r[0]) + __uint_as_float(r[1]);
}
DEVI v4u tr_frag(unsigned addr, unsigned step) {
  s16x4 a = __builtin_amdgcn_ds_read_tr16_b64_v4i16(lp<s16x4>(addr));
  s16x4 b = __builtin_amdgcn_ds_read_tr16_b64_v4i16(lp<s16x4>(addr + step));
  v2u x = __builtin_bit_cast(v2u, a), y = __builtin_bit_cast(v2u, b);
  return v4u{x.x, x.y, y.x, y.y};
}


DEVI float sum4(f32x4 v) {
  float a = v[0] + v[1], b = v[2] + v[3];
  asm("" : "+v"(a));
  return a + b;
}
#define PIN4(a) asm volatile("" : "+v"(a))
template <int N> DEVI void snooze() {
#pragma unroll
  for (int i = 0; i < N / 100; ++i) __builtin_amdgcn_s_sleep(100);
  if (N % 100) __builtin_amdgcn_s_sleep(N % 100);
}

constexpr int A_DELAY = 30, H1_DELAY = 4, H6_DELAY = 3, WO_DELAY = 6, F_DELAY = 50, PF_CKV = 4;
constexpr int T_Q = 480, T_L = 520, D_S = 150, D_4 = 100, D_5 = 100, D_F = 100, LAG_CAP = 2000;
DEVI unsigned swz(unsigned r) { return (((r ^ ((r >> 3) * 12)) << 1) | (r >> 3)) & 15; }
DEVI float max2(float a, float b) { return __builtin_amdgcn_fmed3f(a, b, __builtin_inff()); }
DEVI float max4(float a, float b, float c, float d) { return max2(max2(a, b), max2(c, d)); }
DEVI float fold16(float a, float b) {
  auto r = __builtin_amdgcn_permlane16_swap(__float_as_uint(a), __float_as_uint(b), false, false);
  return __uint_as_float(r[0]) + __uint_as_float(r[1]);
}
DEVI void l2_pull(rsrc_t rs, unsigned voff, unsigned soff, unsigned lds) {
  asm volatile("s_mov_b32 m0, %0\n\ts_nop 0\n\tbuffer_load_dword %1, %2, %3 offen lds" ::"s"(lds), "v"(voff), "s"(rs), "s"(soff) : "memory");
}
DEVI void raw_barrier() {
  __builtin_amdgcn_s_barrier();
  asm volatile("" ::: "memory");
}
DEVI void wait_hint(rsrc_t rws, unsigned voff, unsigned soff, unsigned tg, bool need = true) {
#pragma nounroll
  for (unsigned sp = 0; sp < SPIN_CAP; ++sp) {
    asm volatile("" ::: "memory");
    if (need) need = __builtin_amdgcn_raw_buffer_load_b32(rws, voff, soff, SC1) != tg;
    if (!__any(need)) break;
    __builtin_amdgcn_s_sleep(1);
  }
}
DEVI int wave_max(int v) {
  v = max(v, (int)dppu<QP_X1>(v));
  v = max(v, (int)dppu<QP_X2>(v));
  v = max(v, (int)dppu<ROR4>(v));
  v = max(v, (int)dppu<ROR8>(v));
  return max(max(__builtin_amdgcn_readlane(v, 0), __builtin_amdgcn_readlane(v, 16)),
             max(__builtin_amdgcn_readlane(v, 32), __builtin_amdgcn_readlane(v, 48)));
}
DEVI bool flags_ok(v4u a, v4u b, unsigned tlo, unsigned thi) { return ((a.x ^ tlo) | (b.x ^ tlo) | (a.y ^ thi) | (b.y ^ thi)) == 0; }
DEVI unsigned now32() { return (unsigned)__builtin_amdgcn_s_memrealtime(); }
DEVI void announce(rsrc_t rws, unsigned so, unsigned tg) { stg8(v2u{now32(), tg}, rws, WS_HINT, so); }
DEVI void paced_wait(rsrc_t rws, unsigned voff, unsigned tg, bool need, unsigned t_ref, unsigned delay, v2u hv = v2u{0, 0}) {
  int late = -0x40000000;
#pragma nounroll
  for (unsigned sp = 0; sp < SPIN_CAP; ++sp) {
    if (need && hv.y == tg) late = (int)(hv.x - t_ref), need = false;
    if (!__any(need)) break;
    asm volatile("" ::: "memory");
    if (sp) __builtin_amdgcn_s_sleep(2);
    if (need) hv = ldg8(rws, WS_HINT + voff, 0);
  }
  const int l = wave_max(late);
#pragma nounroll
  for (unsigned sp = 0; sp < SPIN_CAP && (int)(now32() - t_ref) < l + (int)delay; ++sp) __builtin_amdgcn_s_sleep(1);
}
#define GET_TAG(ev)                                               \
  {                                                               \
    const u64 tag = (((u64)rfl(ev.y) << 32) | rfl(ev.x)) + 1;     \
    tlo = (unsigned)tag, thi = (unsigned)(tag >> 32);             \
    tg = tlo & 0x7FFFFFFFu;                                       \
  }

__global__ void __launch_bounds__(512, 2)
fused_kernel(const u16* a_x, const u16* a_ckv, const u16* a_wq, const u16* a_wuk, const u16* a_wuv, const u16* a_wo, const u16* a_w1,
             const u16* a_w2, u16* a_out, char* a_ws) {
  v8u k0, k1;
  v4u k2;
  u64 t_s;
  asm volatile(
      ".Lfused_pf_begin:\n\ts_load_dwordx8 %1, %4, 0x0\n\ts_load_dwordx8 %2, %4, 0x20\n\ts_load_dwordx4 %3, %4, 0x40\n\ts_memrealtime %0"
      : "=&s"(t_s), "=&s"(k0), "=&s"(k1), "=&s"(k2)
      : "s"(__builtin_amdgcn_kernarg_segment_ptr()));
  if (blockIdx.x >= 128 && threadIdx.x < 32) {
    u64 pc;
    unsigned back, len;
    asm volatile("s_getpc_b64 %0\n.Lfused_pf_here:\n\ts_mov_b32 %1, .Lfused_pf_here-.Lfused_pf_begin\n\ts_mov_b32 %2, .Lfused_pf_end-.Lfused_pf_begin"
                 : "=s"(pc), "=s"(back), "=s"(len));
    asm volatile("s_mov_b32 m0, 0x27000\n\ts_nop 0\n\tbuffer_load_dword %0, %1, %2 offen lds" ::"v"(threadIdx.x * 1024),
                 "s"(mk_rsrc((const void*)(pc - back), len)), "s"(((blockIdx.x >> 3) & 15) * 64)
                 : "memory");
  }
  const unsigned lane = threadIdx.x & 63, w = rfl(threadIdx.x >> 6), blk = blockIdx.x;
  const unsigned g = lane >> 4, q4 = (lane >> 2) & 3, p4 = lane & 3, l15 = lane & 15;
  const v4u sent4 = {SENT, SENT, SENT, SENT};
  const f32x4 z4 = {0, 0, 0, 0};
  const v4u ones = {0x3F803F80u, 0x3F803F80u, 0x3F803F80u, 0x3F803F80u};
  const unsigned tr256 = (8 * g + q4) * 256 + (((2 * w + (p4 >> 1)) ^ ((q4 | ((g & 1) << 2)) << 1)) * 16) + 8 * (p4 & 1);
  const unsigned sw256 = (l15 ^ ((g | (((w >> 1) & 1) << 2)) << 1)) * 16;
  const unsigned xc = blk & 7, yc = (blk >> 3) & 15;
  const bool roleA = blk >= 128;
  unsigned tlo, thi, tg;
  {
    unsigned pin0 = tr256, pin1 = sw256;
    asm volatile("s_waitcnt lgkmcnt(0)" : "+s"(t_s), "+s"(k0), "+s"(k1), "+s"(k2) : "v"(pin0), "v"(pin1));
  }
#define KPTR(T, v, i) ((T*)(((u64)(v)[2 * (i) + 1] << 32) | (v)[2 * (i)]))
  const u16 *x = KPTR(const u16, k0, 0), *ckv = KPTR(const u16, k0, 1), *wq = KPTR(const u16, k0, 2), *wuk = KPTR(const u16, k0, 3);
  const u16 *wuv = KPTR(const u16, k1, 0), *wo = KPTR(const u16, k1, 1), *w1 = KPTR(const u16, k1, 2), *w2 = KPTR(const u16, k1, 3);
  u16* out = KPTR(u16, k2, 0);
  char* ws = KPTR(char, k2, 1);
  const rsrc_t rws = mk_rsrc(ws, WS_BYTES);
  const unsigned t_start = (unsigned)t_s;

  if (!roleA) {
    constexpr unsigned L_WUK = 0, L_RED = 0, L_X = 0x8000, L_WO = 0x8000, L_WQ = 0x18000, L_WUV = 0x18000, L_QB = 0x22000, L_QA = 0x23000,
                       L_XC = 0x24000, L_PF = 0x27000, L_CX = 0x2000, L_CNT = 0x23800;
    const unsigned h = xc * 4 + (yc >> 2), j = yc & 3;
    const unsigned hq = h ^ 1;
    const bool p5 = yc < 10;
    const rsrc_t rq = mk_rsrc(wq, 640 * 4096 * 2);
    const unsigned qv = g * 8192 + sw256, qs = (160 * j + 4 * w) * 8192 + hq * 256, ql = L_WQ + w * 1024;
    {
      const unsigned n = w * 64 + lane, row = n / 82, ch = n - row * 82;
      dma(mk_rsrc(x, 4 * 640 * 2), ch < 80 ? row * 1280 + ch * 16 : 0x10000u, 0, L_X + w * 1024);
    }
    v2u ev = ld8_sc1_asm(rws, 0, 0);
#pragma unroll
    for (int i = 0; i < 5; ++i) dma_nt(rq, qv, qs + i * 32 * 8192, ql + i * 8192);
    stg4(SENT, rws, WS_QG + lane * 4, hq * 8192 + j * 2048 + w * 256);
    asm volatile("s_waitcnt vmcnt(6)" : "+v"(ev)::"memory");
    GET_TAG(ev)
    raw_barrier();
    v4u xa[5];
#pragma unroll
    for (int i = 0; i < 5; ++i) xa[i] = *lp<v4u>(L_X + q4 * 1312 + ((p4 + j) & 3) * 320 + g * 16 + i * 64);
    float mean, rstd;
    {
      f32x4 cg = z4, cm = z4;
#pragma unroll
      for (int i = 0; i < 5; ++i) {
        cg = mfma16(xa[i], xa[i], cg);
        cm = mfma16(xa[i], ones, cm);
        xa[i] = p4 == 1 ? ones : xa[i];
      }
      float sq = p4 & 1 ? (p4 & 2 ? cg[3] : cg[1]) : (p4 & 2 ? cg[2] : cg[0]);
      sq = quad_sum(q4 == g ? sq : 0.f);
      sq += dpp<ROR4>(sq);
      sq += dpp<ROR8>(sq);
      mean = sum4(cm) * (1.f / 640.f);
      rstd = __builtin_amdgcn_rsqf(fmaxf(sq * (1.f / 640.f) - mean * mean, 0.f) + 1e-5f);
    }
    unsigned qgo = WS_QG + g * 64 + (l15 >> 1) * 8 + ((lane & 1) << 30), twq = L_WQ + tr256;
    asm volatile("" : "+v"(mean), "+v"(rstd), "+v"(xa[0]), "+v"(xa[1]), "+v"(xa[2]), "+v"(xa[3]), "+v"(xa[4]), "+v"(qgo), "+v"(twq));
    {
      const rsrc_t ruk = mk_rsrc(wuk, 512 * 4096 * 2);
      const unsigned so = (128 * j + 16 * w) * 8192 + h * 256, ld = L_WUK + w * 4096;
#pragma unroll
      for (int e = 0; e < 4; ++e) dma_nt(ruk, (4 * e + g) * 8192 + ((l15 ^ (4 * e + g)) * 16), so, ld + e * 1024);
    }
    VMWAIT(5);
    raw_barrier();
    {
      v4u wf[5];
#pragma unroll
      for (int i = 0; i < 5; ++i) wf[i] = tr_frag(twq + i * 8192, 1024);
      __builtin_amdgcn_sched_barrier(0);
      const f32x4 a = mfma16(xa[4], wf[4], mfma16(xa[2], wf[2], mfma16(xa[0], wf[0], z4))), a2 = mfma16(xa[3], wf[3], mfma16(xa[1], wf[1], z4));
      const float r = ((a[0] + a2[0]) - mean * (a[1] + a2[1])) * rstd;
      const float o = dpp<QP_X1>(r);
      stg8(v2u{pk_bf16(r, o), tg}, rws, qgo, hq * 8192 + j * 2048 + w * 256);
    }
    VMWAIT(1);
    const unsigned pvo = (lane >> 5) * 0x20000 + (lane & 31) * 128, pso = ((xc >> 1) * 4096 + (((xc & 1) << 4) | yc) * 4) * 1024;
    v4u uk[4];
#pragma unroll
    for (int k = 0; k < 4; ++k) uk[k] = *lp<v4u>(L_WUK + w * 4096 + l15 * 256 + (((4 * k + g) ^ l15) * 16));
    {
      v4u g0;
      snooze<H1_DELAY>();
      bool fok = w != 7;
      int lag = 0;
#pragma nounroll
      for (unsigned sp = 0; sp < SPIN_CAP; ++sp) {
        asm volatile("" ::: "memory");
        g0 = ldg16(rws, WS_QG + lane * 16, h * 8192 + w * 1024);
        if (!fok) {
          const v4u a = ldg16(rws, WS_FLAGS + lane * 128, 0), b = ldg16(rws, WS_FLAGS + lane * 128, 8192);
          fok = flags_ok(a, b, tlo, thi);
          lag = max((int)(a.z - t_start), (int)(b.z - t_start));
        }
        if (__all(fok && g0.y == tg && g0.w == tg)) break;
        __builtin_amdgcn_s_sleep(1);
      }
      if (w == 7) *lp<int>(L_CNT + 16) = min(wave_max(lag), LAG_CAP);
      const unsigned col = 4 * q4 + (w >> 1);
      *lp<v2u>(L_QB + col * 256 + (((8 * (w & 1) + 2 * g + (p4 >> 1)) ^ col) * 16) + (p4 & 1) * 8) = v2u{g0.x, g0.z};
      barrier();
      v4u qf[4];
#pragma unroll
      for (int k = 0; k < 4; ++k) qf[k] = *lp<v4u>(L_QB + l15 * 256 + (((4 * k + g) ^ l15) * 16));
      const f32x4 a = mfma16(qf[2], uk[2], mfma16(qf[0], uk[0], z4)) + mfma16(qf[3], uk[3], mfma16(qf[1], uk[1], z4));
      constexpr float SC = 0.08838834764831845f * 1.4426950408889634f;
      const float r = sum4(a) * SC;
      const float o = dpp<QP_X1>(r);
      if ((lane & 1) == 0) *lp<unsigned>(L_QA + g * 256 + (((2 * w + (l15 >> 3)) ^ (h & 15)) * 16) + (l15 & 7) * 2) = san(pk_bf16(r, o));
    }
    barrier();
    if (w == 0) stg16(*lp<v4u>(L_QA + lane * 16), rws, WS_QABS + g * 32768 + l15 * 16, j * 8192 + h * 256);
    if (w - 1 < PF_CKV) l2_pull(mk_rsrc(ckv, 4 * 4096 * 512 * 2), pvo, pso + (32 - 2 * w) * 0x20000, L_PF + w * 256);
    {
      const int gate = (int)rfl(*lp<unsigned>(L_CNT + 16)) + T_L;
      while ((int)(now32() - t_start) < gate) __builtin_amdgcn_s_sleep(2);
    }
    {
      const rsrc_t ruv = mk_rsrc(wuv, 512 * 4096 * 2);
      const unsigned so = (128 * j + w) * 8192 + h * 256, ld = L_WUV + w * 1024;
#pragma unroll
      for (int i = 0; i < 4; ++i) {
        dma_nt(ruv, g * 16 * 8192 + sw256, so + ((i >> 1) * 64 + (i & 1) * 8) * 8192, ld + i * 8192);
      }
    }
    if (w < 5) l2_pull(mk_rsrc(wo, 4096 * 640 * 2), (lane >> 1) * 1280 + (lane & 1) * 128, (xc * 512 + yc * 32) * 1280 + w * 256, L_PF + 0x800 + w * 256);
    if (p5) {
      VMWAIT(0);
      snooze<WO_DELAY>();
      const rsrc_t ro = mk_rsrc(wo, 4096 * 640 * 2);
      const unsigned v0 = (lane >> 3) * 1280 + (((lane & 7) ^ ((g & 1) << 1)) * 16);
      unsigned so = (xc * 512 + w * 64) * 1280 + yc * 128, ld = L_WO + w * 8192;
#pragma unroll 1
      for (int i = 0; i < 4; ++i) {
        dma(ro, v0, so, ld);
        dma(ro, v0 ^ 64, so + 8 * 1280, ld + 1024);
        so += 16 * 1280;
        ld += 2048;
      }
    }
    unsigned xres = ld4_asm(mk_rsrc(x, 4 * 640 * 2), (2 * (w & 1) + (lane >> 5)) * 1280 + (lane & 31) * 4, xc == 0 ? yc * 128 : 0x10000);
    {
      unsigned t = ld4_asm(rws, lane * 2 * OP_STRIDE + 32768, w == 7 ? WS_OP : 0x7F000000u);
      asm volatile("s_waitcnt vmcnt(0)" : "+v"(t), "+v"(xres)::"memory");
    }
    if (w == 0 && lane == 0) *lp<unsigned>(L_CNT) = 0;
    barrier();
    v4u auv[4], aw[4][2];
#pragma unroll
    for (int k = 0; k < 4; ++k) auv[k] = tr_frag(L_WUV + tr256 + k * 8192, 1024);
    if (p5) {
      const unsigned base = L_WO + w * 8192 + (8 * g + q4) * 128 + (((q4 >> 1) | ((g & 1) << 1)) * 32) + p4 * 8;
#pragma unroll
      for (int n = 0; n < 4; ++n)
#pragma unroll
        for (int k = 0; k < 2; ++k) aw[n][k] = tr_frag((base ^ (n << 5)) + k * 4096, 512);
    }
    {
      v4u st;
      const unsigned so = WS_STATS + (w >> 1) * 32 * 512 + h * 16;
      paced_wait(rws, HINT_B + (128 + l15 * 8 + (w & 6) + (g & 1)) * 128, tg, lane < 32, t_start, D_S);
      st = ldg16(rws, (lane & 31) * 512, so);
#pragma nounroll
      for (unsigned sp = 0; sp < SPIN_CAP; ++sp) {
        const bool ok = (st.y == tg && st.w == tg) || lane >= 32;
        if (__all(ok)) break;
        asm volatile("" ::: "memory");
        __builtin_amdgcn_s_sleep(1);
        if (!ok) st = ldg16(rws, (lane & 31) * 512, so);
      }
      asm volatile("" ::: "memory");
      v4u o[4];
      const unsigned vo = g * OP_STRIDE + l15 * 16, so2 = WS_OP + 16 * w * OP_STRIDE + h * 1024 + j * 256;
#pragma unroll
      for (int i = 0; i < 4; ++i) o[i] = __builtin_amdgcn_raw_buffer_load_b128(rws, vo, so2 + 4 * i * OP_STRIDE, SC1 | NT);
      __builtin_amdgcn_sched_barrier(0);
      if (lane == 0 && __hip_atomic_fetch_add(lp<unsigned>(L_CNT), 1u, __ATOMIC_RELAXED, __HIP_MEMORY_SCOPE_WORKGROUP) == 7) announce(rws, blk * 128, tg);
      const float m = __uint_as_float(st.x), l = __uint_as_float(st.z);
      float M = max2(m, dpp<QP_X1>(m));
      M = max2(M, dpp<QP_X2>(M));
      M = max2(M, dpp<ROR4>(M));
      M = max2(M, dpp<ROR8>(M));
      {
        auto r = __builtin_amdgcn_permlane16_swap(__float_as_uint(M), __float_as_uint(M), false, false);
        M = max2(__uint_as_float(r[0]), __uint_as_float(r[1]));
      }
      const float wg = __builtin_amdgcn_exp2f(m - M);
      float L = quad_sum(l * wg);
      L += dpp<ROR4>(L);
      L += dpp<ROR8>(L);
      L = sum16(L);
      const float co = wg * __builtin_amdgcn_rcpf(L);
      float cf[4];
#pragma unroll
      for (int i = 0; i < 4; ++i) {
        cf[i] = __int_as_float(__builtin_amdgcn_ds_bpermute((16 * (w & 1) + 4 * i + g) * 4, __float_as_int(co)));
        PIN4(cf[i]);
      }
      f32x2 t[4] = {};
#pragma unroll
      for (int i = 0; i < 4; ++i)
#pragma unroll
        for (int e = 0; e < 4; ++e) t[e] += cf[i] * f32x2{bf_lo(o[i][e]), bf_hi(o[i][e])};
      *lp<v4u>(L_XC + w * 1024 + g * 256 + ((l15 ^ (4 * (w >> 1) + g)) * 16)) =
          v4u{pk_bf16(t[0].x, t[0].y), pk_bf16(t[1].x, t[1].y), pk_bf16(t[2].x, t[2].y), pk_bf16(t[3].x, t[3].y)};
    }
    barrier();
    const unsigned h4o = (((w >> 1) * 4 + p4) * 8 + xc) * 128;
    v2u h4 = ld8_sc1_asm(rws, WS_HINT + h4o, 0);
    {
      f32x4 a0 = z4, a1 = z4;
      const unsigned rb = L_XC + q4 * 2048 + p4 * 256;
      v4u xf[8];
#pragma unroll
      for (int k = 0; k < 4; ++k) {
        const unsigned co = ((4 * k + g) ^ l15) * 16;
        xf[2 * k] = *lp<v4u>(rb + co);
        xf[2 * k + 1] = *lp<v4u>(rb + 1024 + co);
      }
      __builtin_amdgcn_sched_barrier(0);
#pragma unroll
      for (int k = 0; k < 4; ++k) {
        a0 = mfma16(xf[2 * k], auv[k], a0);
        a1 = mfma16(xf[2 * k + 1], auv[k], a1);
      }
      const float r = sum4(a0 + a1);
      const float o = dpp<QP_X1>(r);
      if ((lane & 1) == 0) stg4(san(pk_bf16(r, o)), rws, WS_CTX + g * 32 + l15 * 2, ((h * 4 + j) * 8 + w) * 128);
    }
    asm volatile("s_waitcnt vmcnt(1)" : "+v"(h4)::"memory");
    if (p5) {
      paced_wait(rws, h4o, tg, lane < 4, t_start, D_4, h4);
      if (lane == 0 && __hip_atomic_fetch_add(lp<unsigned>(L_CNT), 1u, __ATOMIC_RELAXED, __HIP_MEMORY_SCOPE_WORKGROUP) == 15) announce(rws, HINT_B + blk * 128, tg);
      const unsigned vo = WS_CTX + (lane >> 5) * 1024 + (((lane & 31) ^ (lane >> 5)) * 16), so = (4 * xc + (w >> 1)) * 4096 + (w & 1) * 512;
      const unsigned cl = L_CX + w * 2048, cr = cl + p4 * 512 + ((((g >> 1) * 8 + q4 * 2 + (g & 1)) ^ ((p4 & 1) | ((p4 >> 1) << 3))) * 16);
      v4u c0, c1;
      bool n0 = true, n1 = true;
#pragma nounroll
      for (unsigned sp = 0; sp < SPIN_CAP; ++sp) {
        if (n0) dma_sc1(rws, vo, so, cl);
        if (n1) dma_sc1(rws, vo ^ 128, so + 2048, cl + 1024);
        VMWAIT(0);
        c0 = *lp<v4u>(cr), c1 = *lp<v4u>(cr + 256);
        const u64 bad = __ballot(max(umax4(c0), umax4(c1)) == SENT);
        if (bad == 0) break;
        n0 = (bad & 0x3333333333333333ull) != 0, n1 = (bad & 0xCCCCCCCCCCCCCCCCull) != 0;
      }
      f32x4 a[4];
#pragma unroll
      for (int n = 0; n < 4; ++n) a[n] = mfma16(c0, aw[n][0], z4);
#pragma unroll
      for (int n = 0; n < 4; ++n) a[n] = mfma16(c1, aw[n][1], a[n]);
#pragma unroll
      for (int n = 0; n < 4; ++n) *lp<float>(L_RED + w * 1024 + g * 256 + n * 64 + l15 * 4) = sum4(a[n]);
      barrier();
      if (w < 2) {
        f32x2 t[8];
#pragma unroll
        for (int i = 0; i < 8; ++i) t[i] = *lp<f32x2>(L_RED + i * 1024 + w * 512 + lane * 8);
        const f32x2 s = ((t[0] + t[1]) + (t[2] + t[3])) + ((t[4] + t[5]) + (t[6] + t[7])) + f32x2{bf_lo(xres), bf_hi(xres)};
        stg4(san(pk_bf16(s.x, s.y)), rws, WS_HP + (g & 1) * 1024 + (lane >> 5) * 512 + l15 * 4, w * 20480 + yc * 2048 + xc * 64);
      }
    } else if (yc < 15 && w < 2) {
    const unsigned nt = (yc - 10) * 8 + xc, bp = w;
    const unsigned grp = lane >> 3, sub = lane & 7;
    v4u f[7];
    v2u hp;
    const unsigned fo = WS_FP + grp * 256 + sub * 16, fs = nt * 54 * 256 + bp * 128;
    const unsigned f6 = WS_FP + (grp < 6 ? 48 + grp : 53) * 256 + sub * 16;
    const unsigned ho = WS_HP + (8 * (sub >> 2) + grp) * 64 + (sub & 3) * 8, hs = bp * 20480 + (nt >> 1) * 1024 + (nt & 1) * 32;
    {
      const unsigned k = lane >> 1;
      const unsigned ho6 = (128 + (4 * (k >> 3) + 2 * (lane & 1) + bp) * 8 + (k & 7)) * 128;
      wait_hint(rws, WS_HINT + 4 + ho6, 0, tg, lane < 54);
    }
    snooze<H6_DELAY>();
    hp = ldg8(rws, ho, hs);
#pragma unroll
    for (int i = 0; i < 6; ++i) f[i] = ldg16(rws, fo + i * 2048, fs);
    f[6] = ldg16(rws, f6, fs);
#pragma nounroll
    for (unsigned sp = 0; sp < SPIN_CAP; ++sp) {
      unsigned mx[7], m = max(hp.x, hp.y);
      const bool bh = m == SENT;
#pragma unroll
      for (int i = 0; i < 7; ++i) mx[i] = umax4(f[i]), m = max(m, mx[i]);
      if (__all(m != SENT)) break;
      asm volatile("" ::: "memory");
      if (bh) hp = ldg8(rws, ho, hs);
#pragma unroll
      for (int i = 0; i < 6; ++i)
        if (mx[i] == SENT) f[i] = ldg16(rws, fo + i * 2048, fs);
      if (mx[6] == SENT) f[6] = ldg16(rws, f6, fs);
    }
    f32x4 s4 = f32x4{bf_lo(hp.x), bf_hi(hp.x), bf_lo(hp.y), bf_hi(hp.y)};
    if (grp < 6) s4 += __builtin_bit_cast(f32x4, f[6]);
    s4 += ((__builtin_bit_cast(f32x4, f[0]) + __builtin_bit_cast(f32x4, f[1])) + (__builtin_bit_cast(f32x4, f[2]) + __builtin_bit_cast(f32x4, f[3]))) +
          (__builtin_bit_cast(f32x4, f[4]) + __builtin_bit_cast(f32x4, f[5]));
#pragma unroll
    for (int e = 0; e < 4; ++e) s4[e] = sum32(sum16(s4[e] + dpp<ROR8>(s4[e])));
    if (lane < 8)
      stg8(v2u{pk_bf16(s4[0], s4[1]), pk_bf16(s4[2], s4[3])}, mk_rsrc(out, 4 * 640 * 2), (sub >> 2) * 1280 + (sub & 3) * 8, bp * 2560 + nt * 32);
    if (nt == 0 && bp == 0 && lane == 0) stg8(v2u{tlo, thi}, rws, WS_EPOCH, 0);
  }
  } else {
    constexpr unsigned L_QAB = 0x20000, L_RED = 0, L_PB = 0x4000, L_MX = 0x6000, L_LS = 0x6800, L_W2 = 0x8000, L_W1 = 0x12000,
                       L_CS = 0x1D000, L_ST = 0x1D400, L_HB = 0x20000;
    const unsigned b = xc >> 1, s = ((xc & 1) << 4) | yc, p = b * 32 + s;
    const unsigned kf = xc + 8 * (yc >> 2), fcu = 2 * kf + ((yc >> 1) & 1), be = yc & 1;
    const bool ffn = kf < 27;
    const unsigned l31 = lane & 31, hi = lane >> 5;
    v2u ev = ld8_sc1_asm(rws, 0, 0);
    if (w == 7) {
      const unsigned a = blk - 128;
      stg16(sent4, rws, WS_QABS + lane * 16, a * 1024);
      stg16(sent4, rws, WS_CTX + lane * 16, a * 1024);
      if (a < 40) stg16(sent4, rws, WS_HP + lane * 16, a * 1024);
      stg16(sent4, rws, lane < 32 ? WS_STATS + p * 512 + lane * 16 : WS_HINT + ((lane >> 3) & 1) * HINT_B + (a + (g & 1) * 128) * 128 + (lane & 7) * 16, 0);
      if (ffn) {
#pragma unroll
        for (int i = 0; i < 5; ++i) stg16(sent4, rws, WS_FP + (lane >> 3) * (54 * 256) + (lane & 7) * 16, (i * 8 * 54 + fcu) * 256 + be * 128);
      }
    }
    const rsrc_t rc = mk_rsrc(ckv, 4 * 4096 * 512 * 2);
    const unsigned cso = (b * 4096 + (w * 32 + s) * 4) * 1024, cld = w * 4096;
    const unsigned qso = WS_QABS + b * 32768 + w * 4096, qld = L_QAB + w * 4096;
    unsigned ve[4];
#pragma unroll
    for (int e = 0; e < 4; ++e) ve[e] = (lane ^ swz((4 * w + e) & 15)) * 16;
    asm volatile("s_cmp_eq_u32 %1, 7\n\ts_cbranch_scc0 1f\n\ts_waitcnt vmcnt(0)\n1:" : "+v"(ev) : "s"(w) : "scc", "memory");
    if (w == 7) {
      GET_TAG(ev)
      if (lane == 0) stg16(v4u{tlo, thi, t_start, 0}, rws, WS_FLAGS, (blk - 128) * 128);
      l2_pull(rws, 32768, WS_OP + p * OP_STRIDE, 0x26000);
    } else {
      snooze<A_DELAY>();
    }
#pragma unroll
    for (int e = 0; e < 8; ++e) dma_nt(rc, ve[e & 3], cso + (e >> 2) * 0x100000 + (e & 3) * 1024, cld + (e >> 2) * 32768 + (e & 3) * 1024);
    asm volatile("s_waitcnt vmcnt(8)" : "+v"(ev)::"memory");
    GET_TAG(ev)
    v4u fa = {0, 0, 0, 0};
    const unsigned fo = WS_FLAGS + (b * 32 + l31) * 128;
    asm volatile("s_cmp_eq_u32 %3, 7\n\ts_cbranch_scc0 1f\n\tbuffer_load_dwordx4 %0, %1, %2, 0 offen sc1\n1:"
                 : "+v"(fa)
                 : "v"(fo), "s"(rws), "s"(w)
                 : "scc", "memory");
#pragma unroll
    for (int e = 0; e < 4; ++e) dma_nt(rc, ve[e], cso + 0x200000 + e * 1024, cld + 65536 + e * 1024);
    asm volatile("s_cmp_eq_u32 %1, 7\n\ts_cbranch_scc0 1f\n\ts_waitcnt vmcnt(4)\n1:" : "+v"(fa) : "s"(w) : "scc", "memory");
    if (w == 7) {
#pragma nounroll
      for (unsigned sp = 0; sp < SPIN_CAP && !__all(flags_ok(fa, fa, tlo, thi)); ++sp) {
        asm volatile("" ::: "memory");
        if (!flags_ok(fa, fa, tlo, thi)) fa = ldg16(rws, fo, 0);
      }
      *lp<int>(L_QAB + 0x7F00) = min(wave_max((int)(fa.z - t_start)), LAG_CAP);
    }
    barrier();
    const int q_gate = (int)rfl(*lp<unsigned>(L_QAB + 0x7F00)) + T_Q;
    raw_barrier();
    {
      while ((int)(now32() - t_start) < q_gate) __builtin_amdgcn_s_sleep(1);
#pragma unroll
      for (int i = 0; i < 4; ++i) dma_sc1(rws, lane * 16, qso + i * 1024, qld + i * 1024);
#pragma unroll
      for (int e = 0; e < 4; ++e) dma_nt(rc, ve[e], cso + 0x300000 + e * 1024, cld + 98304 + e * 1024);
    }
    VMWAIT(8);
    raw_barrier();
    v4u ka[16], vb[4][4];
    const unsigned kb = (16 * w + l15) * 1024 + ((g ^ swz(l15)) << 4);
    const unsigned va = (4 * g + q4) * 1024 + (w >> 1) * 256 + (((8 * (w & 1) + (p4 >> 1)) ^ swz(4 * g + q4)) << 4) + 8 * (p4 & 1);
    if (w < 6) {
#pragma unroll
      for (int t = 0; t < 16; ++t) ka[t] = *lp<v4u>((kb ^ ((t & 3) << 6)) + (t >> 2) * 256);
    }
#pragma unroll
    for (int sg = 0; sg < 3; ++sg)
#pragma unroll
      for (int t = 0; t < 4; ++t) vb[t][sg] = tr_frag(((sg < 2 ? va : va + 65536) ^ (t << 5)) + (sg & 1) * 32768, 16384);
    VMWAIT(4);
#pragma nounroll
    for (unsigned sp = 0; sp < SPIN_CAP; ++sp) {
      unsigned m = 0;
#pragma unroll
      for (int i = 0; i < 4; ++i) m = max(m, umax4(*lp<v4u>(qld + i * 1024 + lane * 16)));
      if (__all(m != SENT)) break;
#pragma unroll
      for (int i = 0; i < 4; ++i) dma_sc1(rws, lane * 16, qso + i * 1024, qld + i * 1024);
      VMWAIT(0);
    }
    VMWAIT(0);
    barrier();
    const unsigned pu = yc * 3 + w, puk = pu / 10, pur = pu - puk * 10;
    const bool pul = w < 3 && puk < (xc < 3 ? 4u : 3u);
    if (pul) l2_pull(mk_rsrc(w1, 640 * 1728 * 2), lane * 3456, pur * 64 * 3456 + (xc + 8 * puk) * 128, 0x7000 + w * 256);
    if (w >= 6) {
#pragma unroll
      for (int t = 0; t < 16; ++t) ka[t] = *lp<v4u>((kb ^ ((t & 3) << 6)) + (t >> 2) * 256);
    }
    float sc[8], M[2];
    {
      const unsigned base = L_QAB + l15 * 256 + ((g ^ l15) << 4);
      f32x4 a0 = z4, a1 = z4;
      v4u qa[16], qb[16];
#pragma unroll
      for (int t = 0; t < 8; ++t) {
        qa[t] = *lp<v4u>((base ^ ((t & 3) << 6)) + (t >> 2) * 8192);
        qb[t] = *lp<v4u>((base ^ ((t & 3) << 6)) + (t >> 2) * 8192 + 4096);
      }
      __builtin_amdgcn_sched_barrier(0);
#pragma unroll
      for (int t = 0; t < 16; ++t) {
        if (t < 8) {
          qa[t + 8] = *lp<v4u>((base ^ ((t & 3) << 6)) + ((t >> 2) + 2) * 8192);
          qb[t + 8] = *lp<v4u>((base ^ ((t & 3) << 6)) + ((t >> 2) + 2) * 8192 + 4096);
        } else if (t < 12) {
          vb[t - 8][3] = tr_frag(((va + 65536) ^ ((t - 8) << 5)) + 32768, 16384);
        }
        a0 = mfma16(ka[t], qa[t], a0);
        a1 = mfma16(ka[t], qb[t], a1);
        __builtin_amdgcn_sched_barrier(0);
      }
#pragma unroll
      for (int e = 0; e < 4; ++e) sc[e] = a0[e], sc[4 + e] = a1[e];
    }
    {
      const float m0 = max4(sc[0], sc[1], sc[2], sc[3]), m1 = max4(sc[4], sc[5], sc[6], sc[7]);
      auto r = __builtin_amdgcn_permlane16_swap(__float_as_uint(m0), __float_as_uint(m1), false, false);
      const float m = max2(__uint_as_float(r[0]), __uint_as_float(r[1]));
      auto r2 = __builtin_amdgcn_permlane32_swap(__float_as_uint(m), __float_as_uint(m), false, false);
      if (hi == 0) *lp<float>(L_MX + l31 * 48 + w * 4) = max2(__uint_as_float(r2[0]), __uint_as_float(r2[1]));
    }
    barrier();
    {
#pragma unroll
      for (int u = 0; u < 2; ++u) {
        const f32x4 m0 = *lp<f32x4>(L_MX + (l15 + 16 * u) * 48), m1 = *lp<f32x4>(L_MX + (l15 + 16 * u) * 48 + 16);
        M[u] = max2(max4(m0[0], m0[1], m0[2], m0[3]), max4(m1[0], m1[1], m1[2], m1[3]));
      }
#pragma unroll
      for (int e = 0; e < 8; ++e) sc[e] = __builtin_amdgcn_exp2f(sc[e] - M[e >> 2]);
      const unsigned po = L_PB + (w >> 1) * 1024 + lane * 16 + (w & 1) * 8;
      *lp<v2u>(po) = v2u{pk_bf16(sc[0], sc[1]), pk_bf16(sc[2], sc[3])};
      *lp<v2u>(po + 4096) = v2u{pk_bf16(sc[4], sc[5]), pk_bf16(sc[6], sc[7])};
      barrier();
      if (w == 0 && lane == 0) announce(rws, HINT_B + blk * 128, tg);
    }
    {
      v4u pf[8];
#pragma unroll
      for (int sg = 0; sg < 8; ++sg) pf[sg] = *lp<v4u>(L_PB + sg * 1024 + lane * 16);
      __builtin_amdgcn_sched_barrier(0);
      const unsigned vo = g * 4096 + l15 * 8, so = WS_OP + p * OP_STRIDE + w * 128;
      f32x4 o[2][4];
#pragma unroll
      for (int u = 0; u < 2; ++u)
#pragma unroll
        for (int sg = 0; sg < 4; ++sg)
#pragma unroll
          for (int t = 0; t < 4; ++t) o[u][t] = mfma16(pf[4 * u + sg], vb[t][sg], sg ? o[u][t] : z4);
#pragma unroll
      for (int u = 0; u < 2; ++u)
#pragma unroll
        for (int r = 0; r < 4; ++r)
          stg8(v2u{pk_bf16(o[u][0][r], o[u][1][r]), pk_bf16(o[u][2][r], o[u][3][r])}, rws, vo + r * 1024, so + u * 16384);
      const float l = sum32(fold16((sc[0] + sc[1]) + (sc[2] + sc[3]), (sc[4] + sc[5]) + (sc[6] + sc[7])));
      if (hi == 0) *lp<float>(L_LS + l31 * 48 + w * 4) = l;
    }
    VMWAIT(0);
    barrier();
    if (w == 0) {
      const float L = sum4(*lp<f32x4>(L_LS + l31 * 48)) + sum4(*lp<f32x4>(L_LS + l31 * 48 + 16));
      if (hi == 0) stg16(v4u{__float_as_uint(g & 1 ? M[1] : M[0]), tg, __float_as_uint(L), tg}, rws, WS_STATS + l31 * 16, p * 512);
    }
    raw_barrier();
    const unsigned t_pub = now32();
    const unsigned k0 = 4 * (w >> 2) + (w & 3);
    if (ffn) {
      const rsrc_t r1 = mk_rsrc(w1, 640 * 1728 * 2);
      const unsigned voff = (lane >> 2) * 3456 + ((p4 ^ (hi << 1)) * 16);
      unsigned so = k0 * 32 * 3456 + fcu * 64, ld = L_W1 + k0 * 2048;
      snooze<F_DELAY>();
#pragma unroll 1
      for (unsigned i = w >> 2; i < 3; ++i) {
        dma(r1, voff, so, ld);
        dma(r1, voff, so + 16 * 3456, ld + 1024);
        so += 256 * 3456;
        ld += 16384;
      }
    }
    while (now32() - t_pub < D_F) __builtin_amdgcn_s_sleep(2);
    if (pul) l2_pull(mk_rsrc(w2, 1728 * 640 * 2), lane * 128, (xc + 8 * puk) * 64 * 1280 + pur * 8192, 0x7000 + w * 256);
    VMWAIT(0);
    raw_barrier();
    snooze<WO_DELAY>();
    if (ffn) {
      {
        const rsrc_t r2 = mk_rsrc(w2, 1728 * 640 * 2);
        const unsigned s2 = (32 * fcu + 4 * w) * 1280, l2 = L_W2 + w * 1024;
#pragma unroll 1
        for (unsigned i = 0; i < 5; ++i) dma(r2, g * 1280 + sw256, s2 + i * 256, l2 + i * 8192);
      }
      VMWAIT(0);
      raw_barrier();
      const unsigned t1 = L_W1 + k0 * 2048 + (8 * g + q4) * 64 + (g & 1) * 32 + p4 * 8;
      v4u a10[3], a11[3], a2[5];
      const v4u zero4 = {0, 0, 0, 0};
#pragma unroll
      for (int i = 0; i < 3; ++i) {
        if (i < 2 || w < 4) {
          a10[i] = tr_frag(t1 + i * 16384, 256);
          a11[i] = tr_frag((t1 ^ 32) + i * 16384, 256);
        } else {
          a10[i] = zero4, a11[i] = zero4;
        }
      }
#pragma unroll
      for (int i = 0; i < 5; ++i) a2[i] = tr_frag(L_W2 + tr256 + i * 8192, 1024);
      f32x2 cs;
      {
        f32x4 c0 = z4, c1 = z4;
#pragma unroll
        for (int i = 0; i < 3; ++i) {
          c0 = mfma16(ones, a10[i], c0);
          c1 = mfma16(ones, a11[i], c1);
        }
        if (g == 0) {
          *lp<float>(L_CS + w * 128 + l15 * 4) = c0[0];
          *lp<float>(L_CS + w * 128 + 64 + l15 * 4) = c1[0];
        }
        barrier();
        cs = f32x2{0, 0};
#pragma unroll
        for (int i = 0; i < 8; ++i) cs += *lp<f32x2>(L_CS + i * 128 + g * 32 + p4 * 8);
        PIN4(cs);
      }
      const unsigned hv = WS_HP + (lane ^ ((0u - (lane >> 4)) & 3)) * 16, hs = be * 20480 + k0 * 1024;
      const unsigned hl = L_HB + w * 3072;
      unsigned hr = hl + l15 * 64 + ((g ^ ((0u - q4) & 3)) * 16);
      unsigned ro = L_RED + ((g >> 1) * 16 + (g & 1) * 8 + (l15 >> 1)) * 64 + w * 8 + (l15 & 1) * 4, sto = L_ST + (g >> 1) * 64 + (g & 1) * 32 + w * 4;
      unsigned rb = L_RED + ((l15 >> 3) * 16 + g * 4 + p4) * 64 + (p4 << 4), stb = L_ST + (l15 >> 3) * 64 + p4 * 16;
      unsigned fpo = WS_FP + (lane >> 5) * 64 + l15 * 4 + (g & 1) * (8 * 54 * 256);
      v4u on = ones;
      asm volatile("" : "+v"(hr), "+v"(ro), "+v"(sto), "+v"(rb), "+v"(stb), "+v"(fpo), "+v"(on));
      if (w >= 4) *lp<v4u>(hl + 2048 + lane * 16) = zero4;
      if (w == 2) {
        bool need = lane < 40;
#pragma nounroll
        for (unsigned sp = 0; sp < SPIN_CAP && __any(need); ++sp) {
          asm volatile("" ::: "memory");
          if (need) {
            const v4u a = ldg16(rws, WS_FLAGS + lane * 128, 0);
            need = !flags_ok(a, a, tlo, thi);
          }
        }
      }
      if (w < 2) paced_wait(rws, HINT_B + (w * 40 + lane) * 128, tg, lane < 40, t_start, D_5);
      raw_barrier();
      v4u hb[3];
      bool n0 = true, n1 = true, n2 = w < 4;
#pragma nounroll
      for (unsigned sp = 0; sp < SPIN_CAP; ++sp) {
        if (n0) dma_sc1(rws, hv, hs, hl);
        if (n1) dma_sc1(rws, hv, hs + 8192, hl + 1024);
        if (n2) dma_sc1(rws, hv, hs + 16384, hl + 2048);
        VMWAIT(0);
#pragma unroll
        for (int i = 0; i < 3; ++i) hb[i] = *lp<v4u>(hr + i * 1024);
        n0 = __any(umax4(hb[0]) == SENT), n1 = __any(umax4(hb[1]) == SENT), n2 = __any(umax4(hb[2]) == SENT);
        if (!(n0 || n1 || n2)) break;
      }
      if (w == 0 && lane == 0) announce(rws, blk * 128, tg);
      {
        f32x4 c0 = z4, c1 = z4, cm = z4, cg = z4;
#pragma unroll
        for (int i = 0; i < 3; ++i) {
          cg = mfma16(hb[i], hb[i], cg);
          cm = mfma16(hb[i], on, cm);
          c0 = mfma16(hb[i], a10[i], c0);
          c1 = mfma16(hb[i], a11[i], c1);
        }
        float sq = quad_sum((l15 >> 3) == (g >> 1) ? sum4(cg) : 0.f);
        sq += dpp<ROR4>(sq);
        sq += dpp<ROR8>(sq);
        const float sv = fold16(sum4(cm), sq);
        if (l15 == 0) *lp<float>(sto) = sv;
        *lp<float>(ro) = fold16(sum4(c0), sum4(c1));
      }
      barrier();
      {
        const f32x4 st = *lp<f32x4>(stb);
        const f32x4 t4 = (*lp<f32x4>(rb) + *lp<f32x4>(rb ^ 16)) + (*lp<f32x4>(rb ^ 32) + *lp<f32x4>(rb ^ 48));
        const f32x2 t = {t4[0] + t4[2], t4[1] + t4[3]};
        float sx = sum4(st);
        sx += dpp<QP_X1>(sx);
        const float mean = dpp<0x00>(sx) * (1.f / 640.f);
        const float var = fmaxf(dpp<0xAA>(sx) * (1.f / 640.f) - mean * mean, 0.f);
        const float rstd = __builtin_amdgcn_rsqf(var + 1e-5f);
        const float u0 = (t.x - mean * cs.x) * rstd, u1 = (t.y - mean * cs.y) * rstd;
        const float y0 = u0 * __builtin_amdgcn_rcpf(1.f + __builtin_amdgcn_exp2f(-1.4426950408889634f * u0));
        const float y1 = u1 * __builtin_amdgcn_rcpf(1.f + __builtin_amdgcn_exp2f(-1.4426950408889634f * u1));
        const unsigned y = pk_bf16(y0, y1);
        const v4u ah = {dppu<0x00>(y), dppu<0x55>(y), dppu<0xAA>(y), dppu<0xFF>(y)};
        f32x4 dd[5];
#pragma unroll
        for (int i = 0; i < 5; ++i) dd[i] = mfma16(ah, a2[i], z4);
        __builtin_amdgcn_sched_barrier(0);
        const unsigned fs = (w * 54 + fcu) * 256 + be * 128;
        const bool od = g & 1;
        stg4(san(__float_as_uint(od ? dd[1][0] : dd[0][0])), rws, fpo, fs);
        stg4(san(__float_as_uint(od ? dd[3][0] : dd[2][0])), rws, fpo, fs + 16 * 54 * 256);
        if (!od) stg4(san(__float_as_uint(dd[4][0])), rws, fpo, fs + 32 * 54 * 256);
      }
    }
  }
  asm volatile(".Lfused_pf_end:");
}
}

extern "C" void kernel_launch(void* const* d_in, const int* in_sizes, int n_in, void* d_out, int out_size, void* d_ws, size_t ws_size,
                              hipStream_t stream) {
  (void)in_sizes, (void)n_in, (void)out_size, (void)ws_size;
  (void)hipFuncSetAttribute((const void*)fused_kernel, hipFuncAttributeMaxDynamicSharedMemorySize, LDS_BYTES);
  fused_kernel<<<256, 512, LDS_BYTES, stream>>>((const u16*)d_in[0], (const u16*)d_in[1], (const u16*)d_in[2], (const u16*)d_in[3],
                                                (const u16*)d_in[4], (const u16*)d_in[5], (const u16*)d_in[7], (const u16*)d_in[8],
                                                (u16*)d_out, (char*)d_ws);
}
```
